```python
import torch

def _softmax(x, axis=-1):
    return torch.softmax(x, dim=axis)
def _gelu(x):
    return torch.nn.functional.gelu(x, approximate="tanh")
def _silu(x):
    return torch.nn.functional.silu(x)
def _sigmoid(x):
    return torch.sigmoid(x)
def _layernorm(x, axis=-1, eps=1e-5):
    m = x.mean(dim=axis, keepdim=True)
    v = x.var(dim=axis, keepdim=True, unbiased=False)
    return (x - m) / torch.sqrt(v + eps)
def _scatter_add(tgt, idx, src, axis=0):
    return tgt.clone().index_add_(axis, idx, src)
def _segment_sum(src, seg, n):
    out = torch.zeros((n,) + src.shape[1:], dtype=src.dtype)
    return out.index_add_(0, seg, src)
def _pad(x, pw):
    flat = tuple(v for pair in reversed(pw) for v in pair)
    return torch.nn.functional.pad(x, flat)

kernel_name = 'gated_mix__linear_kernel_hedgehog__sliding_local_dilated__prompt'

def setup_inputs():

    _w = (lambda a: (lambda t: t.to(torch.bfloat16) if t.is_floating_point() else t)(torch.as_tensor(a)))
    def _A_setup():
        B, S, D, H = 1, 2048, 640, 32
        Dh = D // H
        return {
            "x": _w((lambda *s: torch.randn(*s, dtype=torch.bfloat16) * 0.05)(B, S, D)),
            "wqkv": _w((lambda *s: torch.randn(*s, dtype=torch.bfloat16) * 1.6)(D, 3 * D)),
            "w_phi": _w((lambda *s: torch.randn(*s, dtype=torch.bfloat16) * 1.6)(H, Dh, Dh) * (Dh ** -0.5)),
            "wo": _w((lambda *s: torch.randn(*s, dtype=torch.bfloat16) * 0.1)(D, D)),
        }
    _A_in = _A_setup()

    def _B_setup():
        B, S, D = 1, 2048, 640
        return {
            "x": _w((lambda *s: torch.randn(*s, dtype=torch.bfloat16) * 0.05)(B, S, D)),
            "wqkv": _w((lambda *s: torch.randn(*s, dtype=torch.bfloat16) * 0.8)(D, 3 * D)),
            "wo": _w((lambda *s: torch.randn(*s, dtype=torch.bfloat16) * 0.05)(D, D)),
        }
    _B_in = _B_setup()

    return {
        "A_x": _A_in["x"],
        "A_wqkv": _A_in["wqkv"],
        "A_w_phi": _A_in["w_phi"],
        "A_wo": _A_in["wo"],
        "B_x": _B_in["x"],
        "B_wqkv": _B_in["wqkv"],
        "B_wo": _B_in["wo"],
        "gate_w": _w((lambda *s: torch.randn(*s, dtype=torch.bfloat16) * 0.05)(640) * 16.0),
    }

def reference(A_x, A_wqkv, A_w_phi, A_wo, B_x, B_wqkv, B_wo, gate_w):
    def _A_ref(x, wqkv, w_phi, wo):
        B, S, D = x.shape
        H = 32; Dh = D // H
        qkv = (lambda x, *d: x.permute(*d))((x @ wqkv).reshape(B, S, 3, H, Dh), 2, 0, 3, 1, 4)
        q, k, v = qkv[0], qkv[1], qkv[2]
        pq = torch.einsum("bhsd,hdf->bhsf", q, w_phi)
        pk = torch.einsum("bhsd,hdf->bhsf", k, w_phi)
        phi_q = torch.concatenate([_softmax(pq, axis=-1), _softmax(-pq, axis=-1)], axis=-1)
        phi_k = torch.concatenate([_softmax(pk, axis=-1), _softmax(-pk, axis=-1)], axis=-1)
        kv = torch.einsum("bhsf,bhsd->bhfd", phi_k, v)
        z  = torch.einsum("bhsf->bhf", phi_k)
        num = torch.einsum("bhqf,bhfd->bhqd", phi_q, kv)
        den = torch.einsum("bhqf,bhf->bhq", phi_q, z)[..., None] + 1e-6
        o = num / den
        return (lambda x, *d: x.permute(*d))(o, 0, 2, 1, 3).reshape(B, S, D) @ wo

    def _B_ref(x, wqkv, wo):
        B, S, D = x.shape
        H = 32; Dh = D // H
        W, dil = 8, 2
        qkv = (lambda x, *d: x.permute(*d))((x @ wqkv).reshape(B, S, 3, H, Dh), 2, 0, 3, 1, 4)
        q, k, v = qkv[0], qkv[1], qkv[2]
        scale = Dh ** -0.5
        pos = torch.arange(S)
        off = torch.arange(W) * dil
        idx = pos[:, None] - off[None, :]
        mask = idx >= 0
        idx_c = torch.where(mask, idx, 0).reshape(-1)
        kg = k[:, :, idx_c, :].reshape(B, H, S, W, Dh)
        vg = v[:, :, idx_c, :].reshape(B, H, S, W, Dh)
        logits = torch.einsum("bhsd,bhswd->bhsw", q, kg) * scale
        logits = torch.where(mask[None, None, :, :], logits, -1e9)
        attn = _softmax(logits, axis=-1)
        o = torch.einsum("bhsw,bhswd->bhsd", attn, vg)
        return (lambda x, *d: x.permute(*d))(o, 0, 2, 1, 3).reshape(B, S, D) @ wo

    x = A_x
    g = _sigmoid((x * gate_w).sum(-1, keepdims=True))
    a = _A_ref(x, A_wqkv, A_w_phi, A_wo)
    b = _B_ref(x, B_wqkv, B_wo)
    return g * a + (1 - g) * b
```

```cpp
#include <hip/hip_runtime.h>
#include <hip/hip_bf16.h>
typedef __hip_bfloat16 bf16;
constexpr int S=2048, D=640, H=32, DH=20;
__device__ inline float b2f(bf16 v){return __bfloat162float(v);}
__device__ inline float rb(float v){return __bfloat162float(__float2bfloat16(v));}

__global__ void gemm_naive(const bf16* A, const bf16* B, float* C, int M, int N, int K, int ldc){
  int n = blockIdx.x*64 + (threadIdx.x&63);
  int m = blockIdx.y*4 + (threadIdx.x>>6);
  if(n>=N||m>=M) return;
  float acc=0;
  for(int k=0;k<K;k++) acc += b2f(A[m*K+k])*b2f(B[k*N+n]);
  C[(size_t)m*ldc+n]=rb(acc);
}
__global__ void gemm_naive_f(const float* A, const bf16* B, float* C, int M, int N, int K){
  int n = blockIdx.x*64 + (threadIdx.x&63);
  int m = blockIdx.y*4 + (threadIdx.x>>6);
  if(n>=N||m>=M) return;
  float acc=0;
  for(int k=0;k<K;k++) acc += A[m*K+k]*b2f(B[k*N+n]);
  C[(size_t)m*N+n]=rb(acc);
}
__global__ void phi_kernel(const float* qkv, const bf16* wphi, float* phiq, float* phik){
  int idx = blockIdx.x*256+threadIdx.x;
  if(idx>=S*H*2) return;
  int which = idx&1; int h=(idx>>1)%H; int s=(idx>>1)/H;
  const float* q = qkv + (size_t)s*1920 + which*640 + h*DH;
  float p[DH]; float mx=-1e30f, mn=1e30f;
  for(int f=0;f<DH;f++){ float a=0; for(int d=0;d<DH;d++) a+=q[d]*b2f(wphi[(h*DH+d)*DH+f]); a=rb(a); p[f]=a; mx=fmaxf(mx,a); mn=fminf(mn,a);}
  float sp=0,sn=0; float ep[DH],en[DH];
  for(int f=0;f<DH;f++){ep[f]=__expf(p[f]-mx); en[f]=__expf(mn-p[f]); sp+=ep[f]; sn+=en[f];}
  float* o = (which?phik:phiq) + ((size_t)s*H+h)*40;
  for(int f=0;f<DH;f++){o[f]=rb(ep[f]/sp); o[20+f]=rb(en[f]/sn);}
}
__global__ void kv_kernel(const float* qkv, const float* phik, float* kv){
  int idx = blockIdx.x*256+threadIdx.x;
  if(idx>=H*40*21) return;
  int d=idx%21, f=(idx/21)%40, h=idx/(21*40);
  float a=0;
  for(int s=0;s<S;s++){ float v = d<20? qkv[(size_t)s*1920+1280+h*DH+d] : 1.f; a+=phik[((size_t)s*H+h)*40+f]*v; }
  kv[idx]=rb(a);
}
__global__ void oa_kernel(const float* phiq, const float* kv, float* oa){
  int idx = blockIdx.x*256+threadIdx.x;
  if(idx>=S*H*DH) return;
  int d=idx%DH, h=(idx/DH)%H, s=idx/(DH*H);
  float num=0,den=0;
  for(int f=0;f<40;f++){ float p=phiq[((size_t)s*H+h)*40+f]; num+=p*kv[(h*40+f)*21+d]; den+=p*kv[(h*40+f)*21+20]; }
  oa[idx]=rb(rb(num)/rb(rb(den)+1e-6f));
}
__global__ void ob_kernel(const float* qkv, float* ob){
  int idx = blockIdx.x*256+threadIdx.x;
  if(idx>=S*H) return;
  int h=idx%H, s=idx/H;
  const float* q = qkv+(size_t)s*1920+h*DH;
  float lg[8]; float mx=-1e30f;
  for(int w=0;w<8;w++){ int t=s-2*w; float a=-1e9f; if(t>=0){ a=0; const float* k=qkv+(size_t)t*1920+640+h*DH; for(int d=0;d<DH;d++) a+=q[d]*k[d]; a=rb(rb(a)*0.22360679775f);} lg[w]=a; mx=fmaxf(mx,a);}
  float sum=0; for(int w=0;w<8;w++){lg[w]=__expf(lg[w]-mx); sum+=lg[w];}
  for(int d=0;d<DH;d++){ float a=0; for(int w=0;w<8;w++){int t=s-2*w; if(t<0)t=0; a+=rb(lg[w]/sum)*qkv[(size_t)t*1920+1280+h*DH+d];} ob[(size_t)s*D+h*DH+d]=rb(a);}
}
__global__ void mix_kernel(const bf16* x, const bf16* gw, const float* a, const float* b, bf16* out){
  int s=blockIdx.x; __shared__ float red[256];
  float acc=0; for(int i=threadIdx.x;i<D;i+=256) acc+=rb(b2f(x[s*D+i])*b2f(gw[i]));
  red[threadIdx.x]=acc; __syncthreads();
  for(int o=128;o>0;o>>=1){ if(threadIdx.x<o) red[threadIdx.x]+=red[threadIdx.x+o]; __syncthreads();}
  float g = rb(1.f/(1.f+__expf(-rb(red[0]))));
  for(int i=threadIdx.x;i<D;i+=256) out[s*D+i]=__float2bfloat16(rb(g*a[s*D+i])+rb(rb(1.f-g)*b[s*D+i]));
}
extern "C" void kernel_launch(void* const* d_in, const int* in_sizes, int n_in, void* d_out, int out_size, void* d_ws, size_t ws_size, hipStream_t stream) {
  const bf16* x=(const bf16*)d_in[0]; const bf16* wA=(const bf16*)d_in[1]; const bf16* wphi=(const bf16*)d_in[2]; const bf16* woA=(const bf16*)d_in[3];
  const bf16* wB=(const bf16*)d_in[5]; const bf16* woB=(const bf16*)d_in[6]; const bf16* gw=(const bf16*)d_in[7];
  float* ws=(float*)d_ws;
  float* qkvA=ws; float* qkvB=qkvA+S*1920; float* phiq=qkvB+S*1920; float* phik=phiq+S*H*40; float* kv=phik+S*H*40; float* oa=kv+H*40*21+64; float* ob=oa+S*D; float* a=ob+S*D; float* b=a+S*D;
  gemm_naive<<<dim3(30,512),256,0,stream>>>(x,wA,qkvA,S,1920,640,1920);
  gemm_naive<<<dim3(30,512),256,0,stream>>>(x,wB,qkvB,S,1920,640,1920);
  phi_kernel<<<S*H*2/256,256,0,stream>>>(qkvA,wphi,phiq,phik);
  kv_kernel<<<(H*40*21+255)/256,256,0,stream>>>(qkvA,phik,kv);
  oa_kernel<<<S*H*DH/256,256,0,stream>>>(phiq,kv,oa);
  ob_kernel<<<S*H/256,256,0,stream>>>(qkvB,ob);
  gemm_naive_f<<<dim3(10,512),256,0,stream>>>(oa,woA,a,S,640,640);
  gemm_naive_f<<<dim3(10,512),256,0,stream>>>(ob,woB,b,S,640,640);
  mix_kernel<<<S,256,0,stream>>>(x,gw,a,b,(bf16*)d_out);
}
```

```cpp
#include <hip/hip_runtime.h>
#include <hip/hip_bf16.h>
#include <stdint.h>

typedef __attribute__((ext_vector_type(4))) short s16x4;
typedef __attribute__((ext_vector_type(8))) short s16x8;
typedef __attribute__((ext_vector_type(8))) __bf16 bf16x8;
typedef __attribute__((ext_vector_type(2))) __bf16 bf16x2;
typedef __attribute__((ext_vector_type(2))) float f32x2;
typedef __attribute__((ext_vector_type(4))) float f32x4;
typedef __attribute__((ext_vector_type(2))) unsigned u32x2;
typedef __attribute__((ext_vector_type(4))) unsigned u32x4;
#define LAS __attribute__((address_space(3)))
#define GAS __attribute__((address_space(1)))
#define DI __device__ __forceinline__

constexpr size_t WS_CNT = 0;
constexpr size_t WS_GATE = 4096;
constexpr size_t WS_PQ = 16384;
constexpr size_t WS_OB = WS_PQ + 2621440;
constexpr size_t WS_OA = WS_OB + 2621440;
constexpr size_t WS_KVP = WS_OA + 2621440;

constexpr float LOG2E = 1.4426950408889634f;

DI unsigned pack2(float a, float b) {
  f32x2 v = {a, b};
  bf16x2 r = __builtin_convertvector(v, bf16x2);
  return __builtin_bit_cast(unsigned, r);
}
DI float rb(float a) { return (float)(__bf16)a; }
DI float bflo(unsigned u) { return __uint_as_float(u << 16); }
DI float bfhi(unsigned u) { return __uint_as_float(u & 0xffff0000u); }
DI float ex2(float a) { return __builtin_amdgcn_exp2f(a); }
DI float rcp(float a) { return __builtin_amdgcn_rcpf(a); }

DI s16x4 trr(unsigned addr) { return __builtin_amdgcn_ds_read_tr16_b64_v4i16((LAS s16x4*)addr); }
DI s16x8 cat8(s16x4 a, s16x4 b) { return __builtin_shufflevector(a, b, 0, 1, 2, 3, 4, 5, 6, 7); }
DI s16x8 ldsr128(unsigned addr) { return *(LAS s16x8*)addr; }
DI s16x4 ldsr64(unsigned addr) { return *(LAS s16x4*)addr; }
DI void ldsw128(unsigned addr, u32x4 v) { *(LAS u32x4*)addr = v; }
DI void ldsw64(unsigned addr, u32x2 v) { *(LAS u32x2*)addr = v; }
DI f32x4 mfma32(s16x8 a, s16x8 b, f32x4 c) {
  return __builtin_amdgcn_mfma_f32_16x16x32_bf16(__builtin_bit_cast(bf16x8, a), __builtin_bit_cast(bf16x8, b), c, 0, 0, 0);
}
DI f32x4 mfma16(s16x4 a, s16x4 b, f32x4 c) {
  const s16x4 z = {0, 0, 0, 0};
  return mfma32(cat8(a, z), cat8(b, z), c);
}
DI s16x8 mk8(u32x2 a, u32x2 b) {
  u32x4 v = {a[0], a[1], b[0], b[1]};
  return __builtin_bit_cast(s16x8, v);
}
DI void glds16(const char* base, unsigned off, unsigned lds) {
  __builtin_amdgcn_global_load_lds(base + off, (LAS void*)lds, 16, 0, 0);
}
DI float xmax4(float v) {
  auto a = __builtin_amdgcn_permlane16_swap(__float_as_uint(v), __float_as_uint(v), false, false);
  v = fmaxf(__uint_as_float(a[0]), __uint_as_float(a[1]));
  auto b = __builtin_amdgcn_permlane32_swap(__float_as_uint(v), __float_as_uint(v), false, false);
  return fmaxf(__uint_as_float(b[0]), __uint_as_float(b[1]));
}
DI float xsum4(float v) {
  auto a = __builtin_amdgcn_permlane16_swap(__float_as_uint(v), __float_as_uint(v), false, false);
  v = __uint_as_float(a[0]) + __uint_as_float(a[1]);
  auto b = __builtin_amdgcn_permlane32_swap(__float_as_uint(v), __float_as_uint(v), false, false);
  return __uint_as_float(b[0]) + __uint_as_float(b[1]);
}
#define WAIT_V(n) asm volatile("s_waitcnt vmcnt(" #n ")" ::: "memory")
#define WAIT_L0() do { asm volatile("" ::: "memory"); __builtin_amdgcn_s_waitcnt(0xc07f); asm volatile("" ::: "memory"); } while (0)
#define BAR() do { asm volatile("" ::: "memory"); __builtin_amdgcn_s_barrier(); asm volatile("" ::: "memory"); } while (0)
#define SBAR() __builtin_amdgcn_sched_barrier(0)
#define SGB(mask, n) __builtin_amdgcn_sched_group_barrier(mask, n, 0)

#define STAMP(i)
#define STAMP2(i)

DI void dual_softmax(const float (&p)[20], unsigned (&ph)[20]) {
  float mx = p[0], mn = p[0];
#pragma unroll
  for (int i = 1; i < 20; ++i) { mx = fmaxf(mx, p[i]); mn = fminf(mn, p[i]); }
  const float bp = -mx * LOG2E, bn = mn * LOG2E;
  float ep[20], en[20], sp = 0.f, sn = 0.f;
#pragma unroll
  for (int i = 0; i < 20; ++i) {
    ep[i] = ex2(__builtin_fmaf(p[i], LOG2E, bp));
    en[i] = ex2(__builtin_fmaf(p[i], -LOG2E, bn));
    sp += ep[i];
    sn += en[i];
  }
  const float rp = rcp(sp), rn = rcp(sn);
#pragma unroll
  for (int i = 0; i < 10; ++i) {
    ph[i] = pack2(ep[2 * i] * rp, ep[2 * i + 1] * rp);
    ph[10 + i] = pack2(en[2 * i] * rn, en[2 * i + 1] * rn);
  }
}

constexpr int K1_WST = 30720;
constexpr int K1_XST = 18432;
constexpr int K1_ST = K1_WST + K1_XST;
constexpr int K1_WPHI = 3 * K1_ST;
constexpr int K1_ZERO = K1_WPHI + 4096;
constexpr int K1_LDS = K1_ZERO + 64;
constexpr int K1_OST = 98304;

template <int NM, int NR, int I>
DI void ilv_t() {
  if constexpr (I < NM) {
    SGB(0x8, 1);
    if constexpr (I + NM < NR) SGB(0x100, 2);
    else if constexpr (I < NR) SGB(0x100, 1);
    ilv_t<NM, NR, I + 1>();
  }
}

template <bool ISB>
DI void k1_consumer_main(unsigned sbase, int w, int lane, f32x4 (&acc)[3][5][2], f32x4 (&acch)[3], char* ws) {
  const int g = lane >> 4, c = lane & 15, qq = (lane >> 2) & 3, pp = lane & 3;
  const unsigned wa0 = sbase + (16 * (g >> 1) + 4 * (g & 1) + qq) * 480 + pp * 40;
  unsigned wh0[3];
#pragma unroll
  for (int j = 0; j < 3; ++j) {
    int ht = w + 4 * j;
    ht = ht > 9 ? 9 : ht;
    wh0[j] = wa0 + (ht >= 5 ? 320 + (ht - 5) * 8 : 160 + ht * 8);
  }
  unsigned xb0[2], xh0[2];
#pragma unroll
  for (int a = 0; a < 2; ++a) {
    xh0[a] = sbase + K1_WST + c * 128 + (((4 * a + g) ^ (c & 7)) * 16);
    xb0[a] = xh0[a] + (16 + 32 * w) * 128;
  }
#pragma unroll
  for (int p = 0; p < 3; ++p)
#pragma unroll
    for (int m = 0; m < 5; ++m)
#pragma unroll
      for (int t = 0; t < 2; ++t) acc[p][m][t] = (f32x4){0.f, 0.f, 0.f, 0.f};
#pragma unroll
  for (int j = 0; j < 3; ++j) acch[j] = (f32x4){0.f, 0.f, 0.f, 0.f};

  s16x8 A[2][6];
  s16x8 B[2][3];
  auto loadA = [&](int buf, unsigned boff, int a, int part) {
#pragma unroll
    for (int m = 0; m < 5; ++m) {
      unsigned ad = wa0 + boff + a * 15360 + part * 160 + m * 8;
      A[buf][m] = cat8(trr(ad), trr(ad + 3840));
    }
    if (ISB) {
      unsigned ad = wh0[part] + boff + a * 15360;
      A[buf][5] = cat8(trr(ad), trr(ad + 3840));
    }
  };
  auto loadB = [&](int buf, unsigned boff, int a) {
    B[buf][0] = ldsr128(xb0[a] + boff);
    B[buf][1] = ldsr128(xb0[a] + boff + 2048);
    if (ISB) B[buf][2] = ldsr128(xh0[a] + boff);
  };
  auto mm = [&](int buf, int bb, int part) {
#pragma unroll
    for (int m = 0; m < 5; ++m) {
      acc[part][m][0] = mfma32(A[buf][m], B[bb][0], acc[part][m][0]);
      acc[part][m][1] = mfma32(A[buf][m], B[bb][1], acc[part][m][1]);
    }
    if (ISB) acch[part] = mfma32(A[buf][5], B[bb][2], acch[part]);
  };
  constexpr int NA = ISB ? 12 : 10, NB = ISB ? 3 : 2, NM = ISB ? 11 : 10;
#define ilv(nr) do { ilv_t<NM, (nr), 0>(); SBAR(); } while (0)
  auto stage = [&](unsigned boff, unsigned nboff, bool last) {
    loadA(1, boff, 0, 1);
    mm(0, 0, 0);
    ilv(NA);
    loadA(0, boff, 0, 2);
    mm(1, 0, 1);
    ilv(NA);
    loadB(1, boff, 1);
    loadA(1, boff, 1, 0);
    mm(0, 0, 2);
    ilv(NA + NB);
    loadA(0, boff, 1, 1);
    mm(1, 1, 0);
    ilv(NA);
    loadA(1, boff, 1, 2);
    mm(0, 1, 1);
    ilv(NA);
    WAIT_L0();
    BAR();
    if (!last) {
      loadB(0, nboff, 0);
      loadA(0, nboff, 0, 0);
      mm(1, 1, 2);
      ilv(NA + NB);
    } else {
      mm(1, 1, 2);
    }
  };
  STAMP(1);
  BAR();
  STAMP(2);
  loadB(0, 0, 0);
  loadA(0, 0, 0, 0);
  SBAR();
  unsigned boff = 0;
#pragma unroll 1
  for (int s = 0; s < 9; ++s) {
    unsigned nboff = boff + K1_ST;
    nboff = nboff >= 3 * K1_ST ? 0 : nboff;
    stage(boff, nboff, false);
    boff = nboff;
  }
  stage(0, 0, true);
}

__global__ void __launch_bounds__(512, 2)
k1_kernel(const char* __restrict__ x, const char* __restrict__ wA, const char* __restrict__ wB,
          const char* __restrict__ wphi, const char* __restrict__ gatew, char* __restrict__ ws) {
  extern __shared__ __attribute__((aligned(16))) char smem[];
  const unsigned sbase = (unsigned)(size_t)(LAS char*)smem;
  const int tid = threadIdx.x, lane = tid & 63;
  const int w = __builtin_amdgcn_readfirstlane(tid >> 6);
  const int b = blockIdx.x, xcd = b & 7, idx = b >> 3;
  const int rt = (xcd & 1) * 8 + (idx & 7);
  const int cg = (xcd >> 1) * 4 + (idx >> 3);
  const bool isB = cg >= 8;
  const int hg = cg & 7;
  const int row0 = rt * 128;
  const int g = lane >> 4, c = lane & 15, qq = (lane >> 2) & 3, pp = lane & 3;
  STAMP(0);
  if (w >= 4) {
    const int lw = w - 4;
    const char* Wb = (isB ? wB : wA) + hg * 160;
    const char* Xb = x;
    unsigned xoff[5], woff[8];
    {
      const int l8 = lane >> 3, slot = lane & 7;
      const unsigned sw = (unsigned)((slot ^ l8) * 16);
#pragma unroll
      for (int i = 0; i < 5; ++i) {
        const int pc = i < 4 ? 4 * i + (lw ^ 2) : 14 + lw;
        int tok = row0 - 16 + pc * 8 + l8;
        tok = tok < 0 ? 0 : tok;
        xoff[i] = (unsigned)tok * 1280u + sw;
      }
    }
    {
      const int u0 = lw * 64 + lane;
      int rho = (u0 * 2185) >> 16;
      int ch = u0 - rho * 30;
#pragma unroll
      for (int i = 0; i < 8; ++i) {
        const int kap = (rho & ~12) | ((rho & 4) << 1) | ((rho & 8) >> 1);
        const int part = (ch >= 10) + (ch >= 20);
        woff[i] = (unsigned)(kap * 3840 + part * (1280 - 160) + ch * 16);
        rho += 8; ch += 16;
        if (ch >= 30) { ch -= 30; rho += 1; }
      }
    }
    const bool extraW = lw < 2;
    const unsigned eoff = extraW ? woff[7] : xoff[4];
    const unsigned elds = extraW ? (unsigned)((28 + lw) * 1024) : (unsigned)(K1_WST + (14 + lw) * 1024);
    auto issue = [&](unsigned boff) {
      const unsigned lb = sbase + boff;
#pragma unroll
      for (int i = 0; i < 4; ++i) glds16(Xb, xoff[i], lb + K1_WST + (4 * i + (lw ^ 2)) * 1024);
#pragma unroll
      for (int i = 0; i < 7; ++i) glds16(Wb, woff[i], lb + (4 * i + lw) * 1024);
      glds16(extraW ? Wb : Xb, eoff, lb + elds);
      Wb += 64 * 3840;
      Xb += 128;
    };
    STAMP(1);
    issue(0);
    issue(K1_ST);
    issue(2 * K1_ST);
    if (lw == 0 && lane < 8) *(LAS unsigned long long*)(sbase + K1_ZERO + lane * 8) = 0ull;
    if (b == 0 && lw == 1 && lane < 16) *(unsigned*)(ws + WS_CNT + lane * 256) = 0u;
    WAIT_V(24);
    STAMP(2);
    BAR();
    unsigned boff = 0;
#pragma unroll 1
    for (int s = 0; s < 7; ++s) {
      WAIT_V(12);
      BAR();
      issue(boff);
      boff += K1_ST;
      boff = boff >= 3 * K1_ST ? 0 : boff;
    }
    WAIT_V(12);
    {
      int e = lw * 64 + lane;
      e = e > 199 ? 199 : e;
      glds16(wphi + hg * 3200, (unsigned)e * 16u, sbase + K1_WPHI + lw * 1024);
    }
    BAR();
    {
      const int tok = row0 + cg * 8 + lw * 2 + (lane >> 5);
      const int l32 = lane & 31;
      const u32x2* xp = (const u32x2*)(x + ((size_t)tok * 640 + l32 * 20) * 2);
      const u32x2* gq = (const u32x2*)(gatew + (l32 * 20) * 2);
      float a = 0.f;
#pragma unroll
      for (int i = 0; i < 5; ++i) {
        u32x2 xv = xp[i], gv = gq[i];
        a += bflo(xv[0]) * bflo(gv[0]) + bfhi(xv[0]) * bfhi(gv[0]) + bflo(xv[1]) * bflo(gv[1]) + bfhi(xv[1]) * bfhi(gv[1]);
      }
#pragma unroll
      for (int o = 16; o >= 1; o >>= 1) a += __shfl_xor(a, o);
      if (l32 == 0) *(float*)(ws + WS_GATE + tok * 4) = 1.f / (1.f + __expf(-a));
    }
    WAIT_V(0);
    WAIT_L0();
    BAR();
    BAR();
  } else {
    f32x4 acc[3][5][2];
    f32x4 acch[3];
    if (isB) {
      k1_consumer_main<true>(sbase, w, lane, acc, acch, ws);
      STAMP(3);
#pragma unroll
      for (int t = 0; t < 2; ++t) {
        const unsigned rowa = sbase + (g * 144 + 16 + 32 * w + 16 * t + c) * 160;
        unsigned pk[3][10];
#pragma unroll
        for (int p = 0; p < 3; ++p)
#pragma unroll
          for (int m = 0; m < 5; ++m) {
            pk[p][2 * m] = pack2(acc[p][m][t][0], acc[p][m][t][1]);
            pk[p][2 * m + 1] = pack2(acc[p][m][t][2], acc[p][m][t][3]);
          }
        ldsw128(rowa + 0, (u32x4){pk[0][0], pk[0][1], pk[0][2], pk[0][3]});
        ldsw128(rowa + 16, (u32x4){pk[0][4], pk[0][5], pk[0][6], pk[0][7]});
        ldsw128(rowa + 32, (u32x4){pk[0][8], pk[0][9], 0u, 0u});
        ldsw128(rowa + 48, (u32x4){0u, 0u, 0u, 0u});
        ldsw128(rowa + 64, (u32x4){pk[1][0], pk[1][1], pk[1][2], pk[1][3]});
        ldsw128(rowa + 80, (u32x4){pk[1][4], pk[1][5], pk[1][6], pk[1][7]});
        ldsw128(rowa + 96, (u32x4){pk[1][8], pk[1][9], pk[2][0], pk[2][1]});
        ldsw128(rowa + 112, (u32x4){pk[2][2], pk[2][3], pk[2][4], pk[2][5]});
        ldsw128(rowa + 128, (u32x4){pk[2][6], pk[2][7], pk[2][8], pk[2][9]});
      }
#pragma unroll
      for (int j = 0; j < 3; ++j) {
        const int ht = w + 4 * j;
        if (ht < 10) {
          const unsigned rowa = sbase + (g * 144 + c) * 160 + 64 + ht * 8;
          ldsw64(rowa, (u32x2){pack2(acch[j][0], acch[j][1]), pack2(acch[j][2], acch[j][3])});
        }
      }
    } else {
      k1_consumer_main<false>(sbase, w, lane, acc, acch, ws);
      STAMP(3);
      u32x2 qk[2][2][5];
      u32x2 vb[2][5];
#pragma unroll
      for (int t = 0; t < 2; ++t)
#pragma unroll
        for (int m = 0; m < 5; ++m) {
#pragma unroll
          for (int p = 0; p < 2; ++p)
            qk[p][t][m] = (u32x2){pack2(acc[p][m][t][0], acc[p][m][t][1]), pack2(acc[p][m][t][2], acc[p][m][t][3])};
          vb[t][m] = (u32x2){pack2(acc[2][m][t][0], acc[2][m][t][1]), pack2(acc[2][m][t][2], acc[2][m][t][3])};
        }
      f32x4 P[2][2][5];
      const unsigned wpa = (pp == g) ? (sbase + K1_WPHI + g * 800 + qq * 40) : (sbase + K1_ZERO);
      const unsigned wst = (pp == g) ? 1u : 0u;
#pragma unroll
      for (int mo = 0; mo < 5; ++mo) {
        const unsigned a0 = wpa + wst * (mo * 8);
        s16x8 W0 = cat8(trr(a0), trr(a0 + wst * 160));
        s16x8 W1 = cat8(trr(a0 + wst * 320), trr(a0 + wst * 480));
        s16x4 W2 = trr(a0 + wst * 640);
#pragma unroll
        for (int p = 1; p >= 0; --p)
#pragma unroll
          for (int t = 0; t < 2; ++t) {
            f32x4 r = (f32x4){0.f, 0.f, 0.f, 0.f};
            r = mfma32(W0, mk8(qk[p][t][0], qk[p][t][1]), r);
            r = mfma32(W1, mk8(qk[p][t][2], qk[p][t][3]), r);
            r = mfma16(W2, __builtin_bit_cast(s16x4, qk[p][t][4]), r);
            P[p][t][mo] = r;
          }
      }
#pragma unroll
      for (int t = 0; t < 2; ++t) {
        const int tok = 32 * w + 16 * t + c;
        u32x2* pqp = (u32x2*)(ws + WS_PQ + ((size_t)(hg * 4 + g) * 2048 + row0 + tok) * 40);
#pragma unroll
        for (int m = 0; m < 5; ++m)
          pqp[m] = (u32x2){pack2(P[0][t][m][0], P[0][t][m][1]), pack2(P[0][t][m][2], P[0][t][m][3])};
        float pk[20];
#pragma unroll
        for (int m = 0; m < 5; ++m)
#pragma unroll
          for (int r = 0; r < 4; ++r) pk[4 * m + r] = P[1][t][m][r];
        unsigned ph[20];
        dual_softmax(pk, ph);
        const unsigned rowa = sbase + (g * 128 + tok) * 160;
#pragma unroll
        for (int i = 0; i < 5; ++i) ldsw128(rowa + 16 * i, (u32x4){ph[4 * i], ph[4 * i + 1], ph[4 * i + 2], ph[4 * i + 3]});
        ldsw128(rowa + 80, (u32x4){vb[t][0][0], vb[t][0][1], vb[t][1][0], vb[t][1][1]});
        ldsw128(rowa + 96, (u32x4){vb[t][2][0], vb[t][2][1], vb[t][3][0], vb[t][3][1]});
        ldsw128(rowa + 112, (u32x4){vb[t][4][0], vb[t][4][1], 0x00003f80u, 0u});
      }
    }
  }
  WAIT_L0();
  STAMP(4);
  BAR();
  STAMP(5);

  if (!isB) {
    const int hl = w >> 1, jt = w & 1;
    const unsigned rbase = sbase + (hl * 128 + 4 * g + qq) * 160 + pp * 8;
    f32x4 kvacc[3];
#pragma unroll
    for (int it = 0; it < 3; ++it) kvacc[it] = (f32x4){0.f, 0.f, 0.f, 0.f};
#pragma unroll
    for (int ks = 0; ks < 4; ++ks) {
      const unsigned r0 = rbase + ks * 32 * 160;
      s16x8 Bv = cat8(trr(r0 + 80 + jt * 32), trr(r0 + 16 * 160 + 80 + jt * 32));
#pragma unroll
      for (int it = 0; it < 3; ++it) {
        s16x8 Ap = cat8(trr(r0 + it * 32), trr(r0 + 16 * 160 + it * 32));
        kvacc[it] = mfma32(Ap, Bv, kvacc[it]);
      }
    }
    const int d = jt * 16 + c;
    if (d < 21) {
      char* kp = ws + WS_KVP + ((size_t)(rt * 32 + hg * 4 + hl) * 21 + d) * 80;
#pragma unroll
      for (int it = 0; it < 3; ++it) {
        const int f = it * 16 + 4 * g;
        if (f < 40) *(u32x2*)(kp + f * 2) = (u32x2){pack2(kvacc[it][0], kvacc[it][1]), pack2(kvacc[it][2], kvacc[it][3])};
      }
    }
  } else {
    const int T = w;
    bool vp[4], vc[4];
#pragma unroll
    for (int r = 0; r < 4; ++r) {
      const int dc = c - (4 * g + r);
      const int dp = dc + 16;
      vc[r] = (dc >= 0) && (dc <= 14) && ((dc & 1) == 0);
      vp[r] = (dp <= 14) && ((dp & 1) == 0) && !(rt == 0 && T == 0);
    }
    const unsigned r0 = sbase + (16 * T) * 160;
    const unsigned ka = r0 + c * 160 + 64 + g * 16;
    const unsigned va = r0 + (4 * g + qq) * 160 + 104 + pp * 8;
    const f32x4 z4 = (f32x4){0.f, 0.f, 0.f, 0.f};
    f32x4 s0[4], s1[4];
    s16x8 V0[4], V1[4];
#pragma unroll
    for (int h = 0; h < 4; ++h) {
      s16x8 Kp = ldsr128(ka + h * 23040);
      s16x8 Kc = ldsr128(ka + h * 23040 + 2560);
      s16x8 Q = ldsr128(ka + h * 23040 + 2560 - 64);
      s0[h] = mfma32(Kp, Q, z4);
      s1[h] = mfma32(Kc, Q, z4);
    }
#pragma unroll
    for (int h = 0; h < 4; ++h) {
      V0[h] = cat8(trr(va + h * 23040), trr(va + h * 23040 + 2560));
      V1[h] = cat8(trr(va + h * 23040 + 32), trr(va + h * 23040 + 2560 + 32));
    }
    constexpr float SC = 0.22360679775f * LOG2E;
    float mx[4];
#pragma unroll
    for (int h = 0; h < 4; ++h) {
      float m = -3.0e38f;
#pragma unroll
      for (int r = 0; r < 4; ++r) {
        s0[h][r] = vp[r] ? s0[h][r] : -3.0e38f;
        s1[h][r] = vc[r] ? s1[h][r] : -3.0e38f;
        m = fmaxf(m, fmaxf(s0[h][r], s1[h][r]));
      }
      mx[h] = m;
    }
#pragma unroll
    for (int h = 0; h < 4; ++h) mx[h] = xmax4(mx[h]);
    float sum[4];
#pragma unroll
    for (int h = 0; h < 4; ++h) {
      const float bb = -mx[h] * SC;
      float sm = 0.f;
#pragma unroll
      for (int r = 0; r < 4; ++r) {
        s0[h][r] = vp[r] ? ex2(__builtin_fmaf(s0[h][r], SC, bb)) : 0.f;
        s1[h][r] = vc[r] ? ex2(__builtin_fmaf(s1[h][r], SC, bb)) : 0.f;
        sm += s0[h][r] + s1[h][r];
      }
      sum[h] = sm;
    }
#pragma unroll
    for (int h = 0; h < 4; ++h) sum[h] = xsum4(sum[h]);
    const unsigned ost = sbase + K1_OST + w * 2560;
#pragma unroll
    for (int h = 0; h < 4; ++h) {
      const float rs = rcp(sum[h]);
      u32x4 pb = {pack2(s0[h][0] * rs, s0[h][1] * rs), pack2(s0[h][2] * rs, s0[h][3] * rs),
                  pack2(s1[h][0] * rs, s1[h][1] * rs), pack2(s1[h][2] * rs, s1[h][3] * rs)};
      s16x8 Pb = __builtin_bit_cast(s16x8, pb);
      f32x4 o0 = mfma32(V0[h], Pb, z4);
      f32x4 o1 = mfma32(V1[h], Pb, z4);
      const unsigned oa = ost + (h >> 1) * 1280 + c * 80 + (h & 1) * 40;
      ldsw64(oa + g * 8, (u32x2){pack2(o0[0], o0[1]), pack2(o0[2], o0[3])});
      if (g == 0) ldsw64(oa + 32, (u32x2){pack2(o1[0], o1[1]), pack2(o1[2], o1[3])});
    }
    WAIT_L0();
#pragma unroll
    for (int i = 0; i < 3; ++i) {
      const int e = i * 64 + lane;
      if (e < 160) {
        const int hp = e >= 80 ? 1 : 0;
        u32x4 v = *(LAS u32x4*)(ost + e * 16);
        *(u32x4*)(ws + WS_OB + ((size_t)(hg * 2 + hp) * 2048 + row0 + 16 * T) * 80 + (e - hp * 80) * 16) = v;
      }
    }
  }
  STAMP(6);
}

constexpr int K2_WST = 25600;
constexpr int K2_OST = 20480;
constexpr int K2_ST = K2_WST + K2_OST;
constexpr int K2_LDS = 3 * K2_ST;
constexpr int K2_PHI = 2 * K2_ST;
constexpr int K2_KV = K2_PHI + 24576;
constexpr int K2_OAS = K2_KV + 4096;

__global__ void __launch_bounds__(512, 2)
k2_kernel(const char* __restrict__ woA, const char* __restrict__ woB, char* __restrict__ ws, char* __restrict__ out) {
  extern __shared__ __attribute__((aligned(16))) char smem[];
  const unsigned sbase = (unsigned)(size_t)(LAS char*)smem;
  const int tid = threadIdx.x, lane = tid & 63;
  const int w = __builtin_amdgcn_readfirstlane(tid >> 6);
  const int b = blockIdx.x, xcd = b & 7, idx = b >> 3;
  const int rt2 = (xcd >> 1) * 8 + (idx & 7);
  const int ct = (xcd & 1) * 4 + (idx >> 3);
  const int g = lane >> 4, c = lane & 15, qq = (lane >> 2) & 3, pp = lane & 3;
  GAS unsigned* cnt = (GAS unsigned*)(ws + WS_CNT);
  STAMP2(0);

  if (w >= 4) {
    const int lw = w - 4;
    unsigned woff[7];
    {
      const int u0 = lw * 64 + lane;
      int rho = (u0 * 6554) >> 16;
      int ch = u0 - rho * 10;
#pragma unroll
      for (int i = 0; i < 6; ++i) {
        const int kap = (rho & ~12) | ((rho & 4) << 1) | ((rho & 8) >> 1);
        woff[i] = (unsigned)(kap * 1280 + ch * 16);
        rho += 25; ch += 6;
        if (ch >= 10) { ch -= 10; rho += 1; }
      }
      const int u = 24 * 64 + lane;
      const int r2 = (u * 6554) >> 16, c2 = u - r2 * 10;
      const int kap = (r2 & ~12) | ((r2 & 4) << 1) | ((r2 & 8) >> 1);
      woff[6] = (unsigned)(kap * 1280 + c2 * 16);
    }
    const unsigned ooff = (unsigned)(lw * 163840 + rt2 * 5120 + lane * 16);
    auto issueW = [&](int s) {
      const char* Wp = (s < 4 ? woB : woA) + (s & 3) * 160 * 1280 + ct * 160;
      const unsigned lb = sbase + (s % 3) * K2_ST;
#pragma unroll
      for (int i = 0; i < 6; ++i) glds16(Wp, woff[i], lb + (4 * i + lw) * 1024);
      if (lw == 0) glds16(Wp, woff[6], lb + 24 * 1024);
    };
    auto issueO = [&](int s) {
      const char* Op = ws + (s < 4 ? WS_OB : WS_OA) + (s & 3) * 4 * 163840;
      const unsigned lb = sbase + (s % 3) * K2_ST + K2_WST + lw * 5120;
#pragma unroll
      for (int i = 0; i < 5; ++i) {
        if (s >= 4) __builtin_amdgcn_global_load_lds(Op + (ooff + i * 1024), (LAS void*)(lb + i * 1024), 16, 0, 16); else
        glds16(Op, ooff + i * 1024, lb + i * 1024);
      }
    };
    STAMP2(1);
    issueO(0); issueW(0);
    issueO(1); issueW(1);
    BAR();
    WAIT_V(11);
    STAMP2(2);
    BAR();
    STAMP2(3);
    issueO(2); issueW(2);
    WAIT_V(11);
    BAR();
    issueO(3); issueW(3);
    WAIT_V(11);
    BAR();
    issueW(4);
    WAIT_V(6);
    BAR();
    issueW(5);
    STAMP2(4);
    {
      GAS unsigned* cp = cnt + (rt2 >> 1) * 64;
      unsigned spins = 0;
      while ((unsigned)__builtin_amdgcn_readfirstlane(__hip_atomic_load(cp, __ATOMIC_RELAXED, __HIP_MEMORY_SCOPE_AGENT)) < 64u) {
        __builtin_amdgcn_s_sleep(1);
        if (++spins > (1u << 22)) break;
      }
    }
    STAMP2(5);
    issueO(4); issueO(5);
    WAIT_V(5);
    BAR();
    STAMP2(6);
    issueO(6); issueW(6);
    WAIT_V(11);
    BAR();
    issueO(7); issueW(7);
    WAIT_V(11);
    BAR();
    WAIT_V(0);
    BAR();
    STAMP2(7);
    return;
  }

  {
    const int hp = xcd * 2 + (idx >> 4), tg = idx & 15;
    const int t = tid;
    const u32x2* pqp = (const u32x2*)(ws + WS_PQ + ((size_t)(hp * 2 + (t >> 7)) * 2048 + tg * 128 + (t & 127)) * 40);
    u32x2 pv[5];
#pragma unroll
    for (int i = 0; i < 5; ++i) pv[i] = pqp[i];
    if (t < 210) {
      const u32x4* kp = (const u32x4*)(ws + WS_KVP) + (size_t)hp * 210 + t;
      u32x4 v[16];
#pragma unroll
      for (int r = 0; r < 16; ++r) v[r] = kp[(size_t)r * 16 * 210];
      float a[8];
#pragma unroll
      for (int j = 0; j < 8; ++j) a[j] = 0.f;
#pragma unroll
      for (int r = 0; r < 16; ++r)
#pragma unroll
        for (int j = 0; j < 4; ++j) { a[2 * j] += bflo(v[r][j]); a[2 * j + 1] += bfhi(v[r][j]); }
      const int row = (t * 52429) >> 18, f8 = t - row * 5;
      ldsw128(sbase + K2_KV + row * 96 + f8 * 16, (u32x4){pack2(a[0], a[1]), pack2(a[2], a[3]), pack2(a[4], a[5]), pack2(a[6], a[7])});
    } else if (t < 252) {
      ldsw128(sbase + K2_KV + (t - 210) * 96 + 80, (u32x4){0u, 0u, 0u, 0u});
    }
    {
      float pk[20];
#pragma unroll
      for (int i = 0; i < 5; ++i) {
        pk[4 * i] = bflo(pv[i][0]); pk[4 * i + 1] = bfhi(pv[i][0]);
        pk[4 * i + 2] = bflo(pv[i][1]); pk[4 * i + 3] = bfhi(pv[i][1]);
      }
      unsigned ph[20];
      dual_softmax(pk, ph);
      const unsigned rowa = sbase + K2_PHI + t * 96;
#pragma unroll
      for (int i = 0; i < 5; ++i) ldsw128(rowa + 16 * i, (u32x4){ph[4 * i], ph[4 * i + 1], ph[4 * i + 2], ph[4 * i + 3]});
      ldsw128(rowa + 80, (u32x4){0u, 0u, 0u, 0u});
    }
    WAIT_L0();
    STAMP2(1);
    BAR();
    STAMP2(2);
    const int r1 = c < 4 ? 16 + c : 20;
    const unsigned oas = sbase + K2_OAS + w * 2560;
    const f32x4 z4 = (f32x4){0.f, 0.f, 0.f, 0.f};
#pragma unroll
    for (int hh = 0; hh < 2; ++hh) {
      const unsigned kb = sbase + K2_KV + hh * 21 * 96;
      s16x8 KA00 = ldsr128(kb + c * 96 + g * 16);
      s16x4 KA01 = ldsr64(kb + c * 96 + 64 + g * 8);
      s16x8 KA10 = ldsr128(kb + r1 * 96 + g * 16);
      s16x4 KA11 = ldsr64(kb + r1 * 96 + 64 + g * 8);
#pragma unroll
      for (int tt = 0; tt < 2; ++tt) {
        const int tok = w * 32 + tt * 16 + c;
        const unsigned pa = sbase + K2_PHI + (hh * 128 + tok) * 96;
        s16x8 PB0 = ldsr128(pa + g * 16);
        s16x4 PB1 = ldsr64(pa + 64 + g * 8);
        f32x4 n0 = mfma32(KA00, PB0, z4);
        n0 = mfma16(KA01, PB1, n0);
        f32x4 n1 = mfma32(KA10, PB0, z4);
        n1 = mfma16(KA11, PB1, n1);
        auto sw = __builtin_amdgcn_permlane16_swap(__float_as_uint(n1[0]), __float_as_uint(n1[0]), false, false);
        const float den = __uint_as_float(sw[1]);
        const float rd = rcp(den + 1e-6f);
        const unsigned oa = oas + (tt * 16 + c) * 80 + hh * 40;
        ldsw64(oa + g * 8, (u32x2){pack2(n0[0] * rd, n0[1] * rd), pack2(n0[2] * rd, n0[3] * rd)});
        if (g == 0) ldsw64(oa + 32, (u32x2){pack2(n1[0] * rd, n1[1] * rd), pack2(n1[2] * rd, n1[3] * rd)});
      }
    }
    WAIT_L0();
    {
      const __amdgpu_buffer_rsrc_t rs = __builtin_amdgcn_make_buffer_rsrc(ws + WS_OA, 0, 2621440, 0x00020000);
      const unsigned gb = (unsigned)((hp * 2048 + tg * 128 + w * 32) * 80);
#pragma unroll
      for (int i = 0; i < 3; ++i) {
        const int e = i * 64 + lane;
        if (e < 160) {
          u32x4 v = *(LAS u32x4*)(oas + e * 16);
          __builtin_amdgcn_raw_buffer_store_b128(v, rs, gb + e * 16, 0, 16);
        }
      }
    }
    STAMP2(3);
    WAIT_V(0);
    STAMP2(4);
    if (lane == 0) __hip_atomic_fetch_add(cnt + tg * 64, 1u, __ATOMIC_RELAXED, __HIP_MEMORY_SCOPE_AGENT);
  }
  const unsigned wa0 = sbase + (16 * (g >> 1) + 4 * (g & 1) + qq) * 160 + pp * 8;
  unsigned ob[5];
#pragma unroll
  for (int ks = 0; ks < 5; ++ks) {
    const int chi = 4 * ks + g, hl = (chi * 13) >> 6, j = chi - hl * 5;
    ob[ks] = sbase + K2_WST + hl * 5120 + (16 * w + c) * 80 + j * 16;
  }
  f32x4 acc[2][5];
#pragma unroll
  for (int hh = 0; hh < 2; ++hh)
#pragma unroll
    for (int m = 0; m < 5; ++m) acc[hh][m] = (f32x4){0.f, 0.f, 0.f, 0.f};
  s16x8 A[2][5], B[2];
  auto loadAB = [&](int buf, int s, int ks) {
    const unsigned bo = (s % 3) * K2_ST;
    B[buf] = ldsr128(ob[ks] + bo);
#pragma unroll
    for (int m = 0; m < 5; ++m) {
      const unsigned ad = wa0 + bo + ks * 32 * 160 + m * 32;
      A[buf][m] = cat8(trr(ad), trr(ad + 8 * 160));
    }
  };
  WAIT_L0();
  BAR();
  STAMP2(5);
  loadAB(0, 0, 0);
#pragma unroll
  for (int s = 0; s < 8; ++s) {
    const int hh = s < 4 ? 0 : 1;
#pragma unroll
    for (int ks = 0; ks < 5; ++ks) {
      const int cur = (s * 5 + ks) & 1;
      if (ks < 4) {
        loadAB(cur ^ 1, s, ks + 1);
      } else if (s < 7) {
        WAIT_L0();
        BAR();
        loadAB(cur ^ 1, s + 1, 0);
      }
#pragma unroll
      for (int m = 0; m < 5; ++m) acc[hh][m] = mfma32(A[cur][m], B[cur], acc[hh][m]);
      SBAR();
    }
  }
  STAMP2(6);
  {
    const int tok = rt2 * 64 + 16 * w + c;
    const float gt = *(const float*)(ws + WS_GATE + tok * 4);
    const float g1 = 1.f - gt;
    char* op = out + ((size_t)tok * 640 + ct * 80 + 4 * g) * 2;
#pragma unroll
    for (int m = 0; m < 5; ++m) {
      float o[4];
#pragma unroll
      for (int r = 0; r < 4; ++r) o[r] = gt * acc[1][m][r] + g1 * acc[0][m][r];
      *(u32x2*)(op + m * 32) = (u32x2){pack2(o[0], o[1]), pack2(o[2], o[3])};
    }
  }
  STAMP2(7);
}

extern "C" void kernel_launch(void* const* d_in, const int* in_sizes, int n_in, void* d_out, int out_size, void* d_ws,
                              size_t ws_size, hipStream_t stream) {
  const char* x = (const char*)d_in[0];
  const char* wA = (const char*)d_in[1];
  const char* wphi = (const char*)d_in[2];
  const char* woA = (const char*)d_in[3];
  const char* wB = (const char*)d_in[5];
  const char* woB = (const char*)d_in[6];
  const char* gw = (const char*)d_in[7];
  (void)hipFuncSetAttribute((const void*)k1_kernel, hipFuncAttributeMaxDynamicSharedMemorySize, K1_LDS);
  (void)hipFuncSetAttribute((const void*)k2_kernel, hipFuncAttributeMaxDynamicSharedMemorySize, K2_LDS);
  k1_kernel<<<256, 512, K1_LDS, stream>>>(x, wA, wB, wphi, gw, (char*)d_ws);
  k2_kernel<<<256, 512, K2_LDS, stream>>>(woA, woB, (char*)d_ws, (char*)d_out);
}
```

```cpp
#include <hip/hip_runtime.h>
#include <hip/hip_bf16.h>
#include <stdint.h>

typedef __attribute__((ext_vector_type(4))) short s16x4;
typedef __attribute__((ext_vector_type(8))) short s16x8;
typedef __attribute__((ext_vector_type(8))) __bf16 bf16x8;
typedef __attribute__((ext_vector_type(2))) __bf16 bf16x2;
typedef __attribute__((ext_vector_type(2))) float f32x2;
typedef __attribute__((ext_vector_type(4))) float f32x4;
typedef __attribute__((ext_vector_type(2))) unsigned u32x2;
typedef __attribute__((ext_vector_type(4))) unsigned u32x4;
#define LAS __attribute__((address_space(3)))
#define GAS __attribute__((address_space(1)))
#define DI __device__ __forceinline__

constexpr size_t WS_CNT = 0;
constexpr size_t WS_GATE = 4096;
constexpr size_t WS_PQ = 16384;
constexpr size_t WS_OB = WS_PQ + 2621440;
constexpr size_t WS_OA = WS_OB + 2621440;
constexpr size_t WS_KVP = WS_OA + 2621440;
constexpr size_t WS_WO = WS_KVP + 860160;

constexpr float LOG2E = 1.4426950408889634f;

DI unsigned pack2(float a, float b) {
  f32x2 v = {a, b};
  bf16x2 r = __builtin_convertvector(v, bf16x2);
  return __builtin_bit_cast(unsigned, r);
}
DI float rb(float a) { return (float)(__bf16)a; }
DI float bflo(unsigned u) { return __uint_as_float(u << 16); }
DI float bfhi(unsigned u) { return __uint_as_float(u & 0xffff0000u); }
DI float ex2(float a) { return __builtin_amdgcn_exp2f(a); }
DI float rcp(float a) { return __builtin_amdgcn_rcpf(a); }
DI float dot2bf(unsigned a, unsigned b, float c) {
  return __builtin_amdgcn_fdot2_f32_bf16(__builtin_bit_cast(bf16x2, a), __builtin_bit_cast(bf16x2, b), c, false);
}

DI s16x4 trr(unsigned addr) { return __builtin_amdgcn_ds_read_tr16_b64_v4i16((LAS s16x4*)addr); }
DI s16x8 cat8(s16x4 a, s16x4 b) { return __builtin_shufflevector(a, b, 0, 1, 2, 3, 4, 5, 6, 7); }
DI s16x8 ldsr128(unsigned addr) { return *(LAS s16x8*)addr; }
DI s16x4 ldsr64(unsigned addr) { return *(LAS s16x4*)addr; }
DI void ldsw128(unsigned addr, u32x4 v) { *(LAS u32x4*)addr = v; }
DI void ldsw64(unsigned addr, u32x2 v) { *(LAS u32x2*)addr = v; }
DI f32x4 mfma32(s16x8 a, s16x8 b, f32x4 c) {
  return __builtin_amdgcn_mfma_f32_16x16x32_bf16(__builtin_bit_cast(bf16x8, a), __builtin_bit_cast(bf16x8, b), c, 0, 0, 0);
}
DI f32x4 mfma16(s16x4 a, s16x4 b, f32x4 c) {
  const s16x4 z = {0, 0, 0, 0};
  return mfma32(cat8(a, z), cat8(b, z), c);
}
DI s16x8 mk8(u32x2 a, u32x2 b) {
  u32x4 v = {a[0], a[1], b[0], b[1]};
  return __builtin_bit_cast(s16x8, v);
}
DI void glds16(const char* base, unsigned off, unsigned lds) {
  __builtin_amdgcn_global_load_lds(base + off, (LAS void*)lds, 16, 0, 0);
}
DI float xmax4(float v) {
  auto a = __builtin_amdgcn_permlane16_swap(__float_as_uint(v), __float_as_uint(v), false, false);
  v = fmaxf(__uint_as_float(a[0]), __uint_as_float(a[1]));
  auto b = __builtin_amdgcn_permlane32_swap(__float_as_uint(v), __float_as_uint(v), false, false);
  return fmaxf(__uint_as_float(b[0]), __uint_as_float(b[1]));
}
DI float xsum4(float v) {
  auto a = __builtin_amdgcn_permlane16_swap(__float_as_uint(v), __float_as_uint(v), false, false);
  v = __uint_as_float(a[0]) + __uint_as_float(a[1]);
  auto b = __builtin_amdgcn_permlane32_swap(__float_as_uint(v), __float_as_uint(v), false, false);
  return __uint_as_float(b[0]) + __uint_as_float(b[1]);
}
#define WAIT_V(n) asm volatile("s_waitcnt vmcnt(" #n ")" ::: "memory")
#define WAIT_L0() do { asm volatile("" ::: "memory"); __builtin_amdgcn_s_waitcnt(0xc07f); asm volatile("" ::: "memory"); } while (0)
#define BAR() do { asm volatile("" ::: "memory"); __builtin_amdgcn_s_barrier(); asm volatile("" ::: "memory"); } while (0)
#define SBAR() __builtin_amdgcn_sched_barrier(0)
#define SGB(mask, n) __builtin_amdgcn_sched_group_barrier(mask, n, 0)

#define STAMP(i)
#define STAMP2(i)

DI void dual_softmax(const float (&p)[20], unsigned (&ph)[20]) {
  float mx = p[0], mn = p[0];
#pragma unroll
  for (int i = 1; i < 20; ++i) { mx = fmaxf(mx, p[i]); mn = fminf(mn, p[i]); }
  const float bp = -mx * LOG2E, bn = mn * LOG2E;
  float ep[20], en[20], sp = 0.f, sn = 0.f;
#pragma unroll
  for (int i = 0; i < 20; ++i) {
    ep[i] = ex2(__builtin_fmaf(p[i], LOG2E, bp));
    en[i] = ex2(__builtin_fmaf(p[i], -LOG2E, bn));
    sp += ep[i];
    sn += en[i];
  }
  const float rp = rcp(sp), rn = rcp(sn);
#pragma unroll
  for (int i = 0; i < 10; ++i) {
    ph[i] = pack2(ep[2 * i] * rp, ep[2 * i + 1] * rp);
    ph[10 + i] = pack2(en[2 * i] * rn, en[2 * i + 1] * rn);
  }
}

constexpr int K1_WH = 15360;
constexpr int K1_XB = 6 * K1_WH;
constexpr int K1_XST = 18432;
constexpr int K1_WPHI = K1_XB + 3 * K1_XST;
constexpr int K1_ZERO = K1_WPHI + 4096;
constexpr int K1_RP = K1_ZERO + 1024;
constexpr int K1_LDS = K1_RP + 10240;
constexpr int K1_OST = 98304;
constexpr int K1_QK = 81920;

constexpr int ftoff(int ft) { return (ft / 5) * 160 + (ft % 5) * 8; }

template <int FH, int HM>
DI void k1_consumer_main(unsigned sbase, int th, int lane, f32x4 (&acc)[8][4], f32x4 (&acch)[8], char* ws) {
  constexpr int NT = FH ? 7 : 8;
  const int g = lane >> 4, c = lane & 15, qq = (lane >> 2) & 3, pp = lane & 3;
  const unsigned wa0 = sbase + (16 * (g >> 1) + 4 * (g & 1) + qq) * 480 + pp * 40;
  unsigned xh0[2], xb0[2];
#pragma unroll
  for (int a = 0; a < 2; ++a) {
    xh0[a] = sbase + K1_XB + c * 128 + (((4 * a + g) ^ (c & 7)) * 16);
    xb0[a] = xh0[a] + (16 + 64 * th) * 128;
  }
#pragma unroll
  for (int m = 0; m < 8; ++m) {
#pragma unroll
    for (int t = 0; t < 4; ++t) acc[m][t] = (f32x4){0.f, 0.f, 0.f, 0.f};
    acch[m] = (f32x4){0.f, 0.f, 0.f, 0.f};
  }
  s16x8 A[2][4], B[2][4], BH[2];
  auto loadA = [&](int grp, unsigned woff) {
#pragma unroll
    for (int m = 0; m < 4; ++m)
      if (4 * grp + m < NT) {
        const unsigned ad = wa0 + woff + ftoff(8 * FH + 4 * grp + m);
        A[grp][m] = cat8(trr(ad), trr(ad + 3840));
      }
  };
  auto loadB = [&](int buf, unsigned xoff) {
#pragma unroll
    for (int t = 0; t < 4; ++t) B[buf][t] = ldsr128(xb0[buf] + xoff + t * 2048);
    if (HM) BH[buf] = ldsr128(xh0[buf] + xoff);
  };
  auto mm = [&](int grp, int buf) {
#pragma unroll
    for (int m = 0; m < 4; ++m)
      if (4 * grp + m < NT) {
#pragma unroll
        for (int t = 0; t < 4; ++t) acc[4 * grp + m][t] = mfma32(A[grp][m], B[buf][t], acc[4 * grp + m][t]);
        if ((HM >> (4 * grp + m)) & 1) acch[4 * grp + m] = mfma32(A[grp][m], BH[buf], acch[4 * grp + m]);
      }
  };
  auto kstep = [&](int a, unsigned woff, unsigned nwoff, unsigned nxoff, bool last) {
    loadA(1, woff);
    mm(0, a);
    SGB(0x8, 1); SGB(0x100, 2); SGB(0x8, 1); SGB(0x100, 2); SGB(0x8, 1); SGB(0x100, 2); SGB(0x8, 1); SGB(0x100, 2);
    SBAR();
    WAIT_L0();
    BAR();
    if (!last) {
      loadB(a ^ 1, nxoff);
      loadA(0, nwoff);
    }
    mm(1, a);
    if (!last) {
      SGB(0x8, 1); SGB(0x100, 2); SGB(0x8, 1); SGB(0x100, 2); SGB(0x8, 1); SGB(0x100, 2); SGB(0x8, 1); SGB(0x100, 2);
      SGB(0x8, 1); SGB(0x100, 2); SGB(0x8, 1); SGB(0x100, 2); SGB(0x8, 1); SGB(0x100, 1);
    }
    SBAR();
  };
  STAMP(1);
  BAR();
  STAMP(2);
  loadB(0, 0);
  loadA(0, 0);
  SBAR();
  unsigned woff = 0, xoff = 0;
#pragma unroll 1
  for (int s = 0; s < 9; ++s) {
    unsigned nw = woff + 2 * K1_WH, nx = xoff + K1_XST;
    if (nw >= 6 * K1_WH) { nw = 0; nx = 0; }
    kstep(0, woff, woff + K1_WH, xoff, false);
    kstep(1, woff + K1_WH, nw, nx, false);
    woff = nw; xoff = nx;
  }
  kstep(0, 0, K1_WH, 0, false);
  kstep(1, K1_WH, 0, 0, true);
}

template <int FH, int HM, bool ISB>
DI void k1_dump(unsigned sbase, int th, int lane, const f32x4 (&acc)[8][4], const f32x4 (&acch)[8]) {
  constexpr int NT = FH ? 7 : 8;
  const int g = lane >> 4, c = lane & 15;
#pragma unroll
  for (int t = 0; t < 4; ++t) {
    u32x2 v[8];
#pragma unroll
    for (int m = 0; m < NT; ++m) v[m] = (u32x2){pack2(acc[m][t][0], acc[m][t][1]), pack2(acc[m][t][2], acc[m][t][3])};
    const int tok = 64 * th + 16 * t + c;
    if (ISB) {
      const unsigned rowa = sbase + (g * 144 + 16 + tok) * 160;
      if (FH == 0) {
        ldsw128(rowa + 0, (u32x4){v[0][0], v[0][1], v[1][0], v[1][1]});
        ldsw128(rowa + 16, (u32x4){v[2][0], v[2][1], v[3][0], v[3][1]});
        ldsw128(rowa + 32, (u32x4){v[4][0], v[4][1], 0u, 0u});
        ldsw128(rowa + 48, (u32x4){0u, 0u, 0u, 0u});
        ldsw128(rowa + 64, (u32x4){v[5][0], v[5][1], v[6][0], v[6][1]});
        ldsw64(rowa + 80, v[7]);
      } else {
        ldsw64(rowa + 88, v[0]);
        ldsw128(rowa + 96, (u32x4){v[1][0], v[1][1], v[2][0], v[2][1]});
        ldsw128(rowa + 112, (u32x4){v[3][0], v[3][1], v[4][0], v[4][1]});
        ldsw128(rowa + 128, (u32x4){v[5][0], v[5][1], v[6][0], v[6][1]});
      }
    } else {
      const unsigned qa = sbase + K1_QK + (4 * th + t) * 5120 + lane * 16;
      if (FH == 0) {
#pragma unroll
        for (int i = 0; i < 4; ++i) ldsw128(qa + i * 1024, (u32x4){v[2 * i][0], v[2 * i][1], v[2 * i + 1][0], v[2 * i + 1][1]});
      } else {
        ldsw128(qa + 4096, (u32x4){v[0][0], v[0][1], v[1][0], v[1][1]});
        const unsigned rowa = sbase + (g * 128 + tok) * 160;
        ldsw128(rowa + 80, (u32x4){v[2][0], v[2][1], v[3][0], v[3][1]});
        ldsw128(rowa + 96, (u32x4){v[4][0], v[4][1], v[5][0], v[5][1]});
        ldsw128(rowa + 112, (u32x4){v[6][0], v[6][1], 0x00003f80u, 0u});
      }
    }
  }
  if (ISB) {
#pragma unroll
    for (int m = 0; m < NT; ++m)
      if ((HM >> m) & 1)
        ldsw64(sbase + (g * 144 + c) * 160 + 64 + (8 * FH + m - 5) * 8, (u32x2){pack2(acch[m][0], acch[m][1]), pack2(acch[m][2], acch[m][3])});
  }
}

template <int FH, int HM, bool ISB>
DI void k1_consumer(unsigned sbase, int th, int lane, char* ws) {
  f32x4 acc[8][4], acch[8];
  k1_consumer_main<FH, HM>(sbase, th, lane, acc, acch, ws);
  STAMP(3);
  k1_dump<FH, HM, ISB>(sbase, th, lane, acc, acch);
}

__global__ void __launch_bounds__(512, 2)
k1_kernel(const char* __restrict__ x, const char* __restrict__ wA, const char* __restrict__ wB,
          const char* __restrict__ wphi, const char* __restrict__ gatew, const char* __restrict__ woA,
          const char* __restrict__ woB, char* __restrict__ ws) {
  extern __shared__ __attribute__((aligned(16))) char smem[];
  const unsigned sbase = (unsigned)(size_t)(LAS char*)smem;
  const int tid = threadIdx.x, lane = tid & 63;
  const int w = __builtin_amdgcn_readfirstlane(tid >> 6);
  const int b = blockIdx.x, xcd = b & 7, idx = b >> 3;
  const int rt = (xcd & 1) * 8 + (idx & 7);
  const int cg = (xcd >> 1) * 4 + (idx >> 3);
  const bool isB = cg >= 8;
  const int hg = cg & 7;
  const int row0 = rt * 128;
  const int g = lane >> 4, c = lane & 15, qq = (lane >> 2) & 3, pp = lane & 3;
  STAMP(0);
  if (w == 7) {
#pragma unroll
    for (int i = 0; i < 4; ++i) {
      int e = i * 64 + lane;
      e = e > 199 ? 199 : e;
      glds16(wphi + hg * 3200, (unsigned)e * 16u, sbase + K1_WPHI + i * 1024);
    }
#pragma unroll
    for (int r = 0; r < 2; ++r) {
      const int u = r == 0 ? b : (b < 64 ? 256 + b : b);
      const int uct = u / 40, uks = u - uct * 40;
      const char* wsrc = (uks < 20 ? woB + (size_t)uks * 40960 : woA + (size_t)(uks - 20) * 40960) + uct * 160;
#pragma unroll
      for (int i = 0; i < 5; ++i) {
        const int e = i * 64 + lane;
        const int rho = (e * 6554) >> 16, ch = e - rho * 10;
        const int kap = (rho & ~12) | ((rho & 4) << 1) | ((rho & 8) >> 1);
        glds16(wsrc, (unsigned)(kap * 1280 + ch * 16), sbase + K1_RP + r * 5120 + i * 1024);
      }
    }
    asm volatile("ds_write_b128 %0, %1" :: "v"(sbase + K1_ZERO + lane * 16), "v"((u32x4){0u, 0u, 0u, 0u}) : "memory");
    if (b == 0 && lane < 16) *(unsigned*)(ws + WS_CNT + lane * 256) = 0u;
    const int gtok = row0 + cg * 8 + (lane >> 3);
    u32x4 xv[10], gv[10];
#pragma unroll
    for (int i = 0; i < 10; ++i) {
      gv[i] = *(const u32x4*)(gatew + (lane & 7) * 160 + i * 16);
      xv[i] = *(const u32x4*)(x + (size_t)gtok * 1280 + (lane & 7) * 160 + i * 16);
    }
    SBAR();
#pragma unroll 1
    for (int i = 0; i < 8; ++i) BAR();
    SBAR();
    {
      float a0 = 0.f, a1 = 0.f;
#pragma unroll
      for (int i = 0; i < 10; ++i)
#pragma unroll
        for (int j = 0; j < 4; j += 2) {
          a0 = dot2bf(xv[i][j], gv[i][j], a0);
          a1 = dot2bf(xv[i][j + 1], gv[i][j + 1], a1);
        }
      float a = a0 + a1;
      BAR();
      a += __int_as_float(__builtin_amdgcn_update_dpp(0, __float_as_int(a), 0xB1, 0xf, 0xf, true));
      a += __int_as_float(__builtin_amdgcn_update_dpp(0, __float_as_int(a), 0x4E, 0xf, 0xf, true));
      a += __int_as_float(__builtin_amdgcn_update_dpp(0, __float_as_int(a), 0x141, 0xf, 0xf, true));
      if ((lane & 7) == 0) *(float*)(ws + WS_GATE + gtok * 4) = rcp(1.f + ex2(-a * LOG2E));
      BAR();
    }
#pragma unroll
    for (int r = 0; r < 2; ++r) {
      const int u = r == 0 ? b : 256 + b;
      const unsigned ra = sbase + K1_RP + r * 5120 + (16 * (g >> 1) + 4 * (g & 1) + qq) * 160 + pp * 8;
      s16x8 F[5];
#pragma unroll
      for (int m = 0; m < 5; ++m) F[m] = cat8(trr(ra + m * 32), trr(ra + m * 32 + 1280));
      BAR();
      if (r == 0 || b < 64) {
#pragma unroll
        for (int m = 0; m < 5; ++m) *(s16x8*)(ws + WS_WO + (size_t)u * 5120 + m * 1024 + lane * 16) = F[m];
      }
      BAR();
    }
#pragma unroll 1
    for (int i = 0; i < 5; ++i) BAR();
    WAIT_L0();
    BAR(); BAR();
  } else if (w >= 4) {
    const int lw = w - 4;
    const char* Wb = (isB ? wB : wA) + hg * 160;
    const char* Xb = x;
    unsigned xoff[6], woff[5];
    {
      const int l8 = lane >> 3, slot = lane & 7;
      const unsigned sw = (unsigned)((slot ^ l8) * 16);
#pragma unroll
      for (int i = 0; i < 6; ++i) {
        int tok = row0 - 16 + (3 * i + lw) * 8 + l8;
        tok = tok < 0 ? 0 : tok;
        xoff[i] = (unsigned)tok * 1280u + sw;
      }
    }
    {
      const int u0 = lw * 64 + lane;
      int rho = (u0 * 2185) >> 16;
      int ch = u0 - rho * 30;
#pragma unroll
      for (int i = 0; i < 5; ++i) {
        const int kap = (rho & ~12) | ((rho & 4) << 1) | ((rho & 8) >> 1);
        const int part = (ch >= 10) + (ch >= 20);
        woff[i] = (unsigned)(kap * 3840 + part * (1280 - 160) + ch * 16);
        rho += 6; ch += 12;
        if (ch >= 30) { ch -= 30; rho += 1; }
      }
    }
    unsigned wl = sbase + lw * 1024, xl = sbase + K1_XB + lw * 1024;
    auto issueW = [&]() {
#pragma unroll
      for (int i = 0; i < 5; ++i) glds16(Wb, woff[i], wl + i * 3072);
      Wb += 32 * 3840;
      wl += K1_WH;
      if (wl >= sbase + K1_XB) wl -= K1_XB;
    };
    auto issueX = [&]() {
#pragma unroll
      for (int i = 0; i < 6; ++i) glds16(Xb, xoff[i], xl + i * 3072);
      Xb += 128;
      xl += K1_XST;
      if (xl >= sbase + K1_WPHI) xl -= 3 * K1_XST;
    };
    STAMP(1);
    issueX(); issueW(); issueW(); issueX(); issueW(); WAIT_V(16);
    STAMP(2);
    BAR();
    issueW(); issueX(); WAIT_V(22); BAR();
    issueW(); issueW(); WAIT_V(21); BAR();
    issueX(); issueW(); issueW(); WAIT_V(32); BAR();
#pragma unroll 1
    for (int i = 0; i < 6; ++i) {
      issueW(); WAIT_V(26); BAR();
      issueX(); issueW(); WAIT_V(32); BAR();
    }
    WAIT_V(21); BAR();
    WAIT_V(16); BAR();
    WAIT_V(5); BAR();
    WAIT_V(0); BAR();
    BAR();
  } else {
    const int th = w & 1;
    if (isB) {
      if (w == 0) k1_consumer<0, 0x60, true>(sbase, th, lane, ws);
      else if (w == 1) k1_consumer<0, 0x80, true>(sbase, th, lane, ws);
      else if (w == 2) k1_consumer<1, 0x0f, true>(sbase, th, lane, ws);
      else k1_consumer<1, 0x70, true>(sbase, th, lane, ws);
    } else {
      if (w < 2) k1_consumer<0, 0, false>(sbase, th, lane, ws);
      else k1_consumer<1, 0, false>(sbase, th, lane, ws);
    }
  }
  WAIT_L0();
  STAMP(4);
  BAR();
  STAMP(5);

  if (!isB) {
    {
      const int tok = 16 * w + c;
      u32x2 in[2][5];
      {
        const unsigned qa = sbase + K1_QK + w * 5120 + lane * 16;
#pragma unroll
        for (int i = 0; i < 5; ++i) {
          const u32x4 v = *(LAS u32x4*)(qa + i * 1024);
          in[(2 * i) / 5][(2 * i) % 5] = (u32x2){v[0], v[1]};
          in[(2 * i + 1) / 5][(2 * i + 1) % 5] = (u32x2){v[2], v[3]};
        }
      }
      f32x4 P[2][5];
      const unsigned wpa = (pp == g) ? (sbase + K1_WPHI + g * 800 + qq * 40) : (sbase + K1_ZERO);
#pragma unroll
      for (int mo = 0; mo < 5; ++mo) {
        const unsigned a0 = wpa + mo * 8;
        s16x8 W0 = cat8(trr(a0), trr(a0 + 160));
        s16x8 W1 = cat8(trr(a0 + 320), trr(a0 + 480));
        s16x4 W2 = trr(a0 + 640);
#pragma unroll
        for (int p = 1; p >= 0; --p) {
          f32x4 r = (f32x4){0.f, 0.f, 0.f, 0.f};
          r = mfma32(W0, mk8(in[p][0], in[p][1]), r);
          r = mfma32(W1, mk8(in[p][2], in[p][3]), r);
          r = mfma16(W2, __builtin_bit_cast(s16x4, in[p][4]), r);
          P[p][mo] = r;
        }
      }
      u32x2* pqp = (u32x2*)(ws + WS_PQ + ((size_t)(hg * 4 + g) * 2048 + row0 + tok) * 40);
#pragma unroll
      for (int m = 0; m < 5; ++m) pqp[m] = (u32x2){pack2(P[0][m][0], P[0][m][1]), pack2(P[0][m][2], P[0][m][3])};
      float pv[20];
#pragma unroll
      for (int m = 0; m < 5; ++m)
#pragma unroll
        for (int r = 0; r < 4; ++r) pv[4 * m + r] = P[1][m][r];
      unsigned ph[20];
      dual_softmax(pv, ph);
      const unsigned rowa = sbase + (g * 128 + tok) * 160;
#pragma unroll
      for (int i = 0; i < 5; ++i) ldsw128(rowa + 16 * i, (u32x4){ph[4 * i], ph[4 * i + 1], ph[4 * i + 2], ph[4 * i + 3]});
    }
    WAIT_L0();
    BAR();
    {
      const int hl = w >> 1, jt = w & 1;
      const unsigned rbase = sbase + (hl * 128 + 4 * g + qq) * 160 + pp * 8;
      f32x4 kvacc[3];
#pragma unroll
      for (int it = 0; it < 3; ++it) kvacc[it] = (f32x4){0.f, 0.f, 0.f, 0.f};
#pragma unroll
      for (int ks = 0; ks < 4; ++ks) {
        const unsigned r0 = rbase + ks * 32 * 160;
        s16x8 Bv = cat8(trr(r0 + 80 + jt * 32), trr(r0 + 16 * 160 + 80 + jt * 32));
#pragma unroll
        for (int it = 0; it < 3; ++it) {
          s16x8 Ap = cat8(trr(r0 + it * 32), trr(r0 + 16 * 160 + it * 32));
          kvacc[it] = mfma32(Ap, Bv, kvacc[it]);
        }
      }
      const int d = jt * 16 + c;
      if (d < 21) {
        char* kp = ws + WS_KVP + ((size_t)(rt * 32 + hg * 4 + hl) * 21 + d) * 80;
#pragma unroll
        for (int it = 0; it < 3; ++it) {
          const int f = it * 16 + 4 * g;
          if (f < 40) *(u32x2*)(kp + f * 2) = (u32x2){pack2(kvacc[it][0], kvacc[it][1]), pack2(kvacc[it][2], kvacc[it][3])};
        }
      }
    }
  } else {
    const int T = w;
    const bool par = (c & 1) != 0;
    const bool cur0 = c - (4 * g + (c & 1)) >= 0, cur1 = c - (4 * g + 2 + (c & 1)) >= 0;
    const bool first = (rt == 0 && T == 0);
    const bool ok0 = cur0 || !first, ok1 = cur1 || !first;
    const unsigned hm = par ? 0xffff0000u : 0x0000ffffu;
    const unsigned mC0 = cur0 ? hm : 0u, mP0 = cur0 ? 0u : hm, mC1 = cur1 ? hm : 0u, mP1 = cur1 ? 0u : hm;
    const unsigned r0 = sbase + (16 * T) * 160;
    const unsigned ka = r0 + c * 160 + 64 + g * 16;
    const unsigned va = r0 + (4 * g + qq) * 160 + 104 + pp * 8;
    const f32x4 z4 = (f32x4){0.f, 0.f, 0.f, 0.f};
    f32x4 s0[4], s1[4];
    s16x8 V0[4], V1[4];
#pragma unroll
    for (int h = 0; h < 4; ++h) {
      s16x8 Kp = ldsr128(ka + h * 23040);
      s16x8 Kc = ldsr128(ka + h * 23040 + 2560);
      s16x8 Q = ldsr128(ka + h * 23040 + 2560 - 64);
      s0[h] = mfma32(Kp, Q, z4);
      s1[h] = mfma32(Kc, Q, z4);
    }
#pragma unroll
    for (int h = 0; h < 4; ++h) {
      V0[h] = cat8(trr(va + h * 23040), trr(va + h * 23040 + 2560));
      V1[h] = cat8(trr(va + h * 23040 + 32), trr(va + h * 23040 + 2560 + 32));
    }
    constexpr float SC = 0.22360679775f * LOG2E;
    float ea[4], eb[4], mx[4], sum[4];
#pragma unroll
    for (int h = 0; h < 4; ++h) {
      const float p0 = par ? s0[h][1] : s0[h][0], p1 = par ? s0[h][3] : s0[h][2];
      const float c0 = par ? s1[h][1] : s1[h][0], c1 = par ? s1[h][3] : s1[h][2];
      float a = cur0 ? c0 : p0, bq = cur1 ? c1 : p1;
      a = ok0 ? a : -3.0e38f;
      bq = ok1 ? bq : -3.0e38f;
      ea[h] = a; eb[h] = bq;
      mx[h] = fmaxf(a, bq);
    }
#pragma unroll
    for (int h = 0; h < 4; ++h) mx[h] = xmax4(mx[h]);
#pragma unroll
    for (int h = 0; h < 4; ++h) {
      const float bb = -mx[h] * SC;
      ea[h] = ok0 ? ex2(__builtin_fmaf(ea[h], SC, bb)) : 0.f;
      eb[h] = ok1 ? ex2(__builtin_fmaf(eb[h], SC, bb)) : 0.f;
      sum[h] = ea[h] + eb[h];
    }
#pragma unroll
    for (int h = 0; h < 4; ++h) sum[h] = xsum4(sum[h]);
    const unsigned ost = sbase + K1_OST + w * 2560;
#pragma unroll
    for (int h = 0; h < 4; ++h) {
      const float rs = rcp(sum[h]);
      const float pa = ea[h] * rs, pbv = eb[h] * rs;
      const unsigned da = pack2(pa, pa), db = pack2(pbv, pbv);
      u32x4 pb = {da & mP0, db & mP1, da & mC0, db & mC1};
      s16x8 Pb = __builtin_bit_cast(s16x8, pb);
      f32x4 o0 = mfma32(V0[h], Pb, z4);
      f32x4 o1 = mfma32(V1[h], Pb, z4);
      const int e0 = h * 20 + 4 * g, e1 = h * 20 + 16;
      ldsw64(ost + (e0 >> 3) * 256 + c * 16 + (e0 & 7) * 2, (u32x2){pack2(o0[0], o0[1]), pack2(o0[2], o0[3])});
      if (g == 0) ldsw64(ost + (e1 >> 3) * 256 + c * 16 + (e1 & 7) * 2, (u32x2){pack2(o1[0], o1[1]), pack2(o1[2], o1[3])});
    }
    WAIT_L0();
#pragma unroll
    for (int i = 0; i < 3; ++i) {
      const int e = i * 64 + lane;
      if (e < 160) {
        u32x4 v = *(LAS u32x4*)(ost + e * 16);
        *(u32x4*)(ws + WS_OB + (size_t)(rt * 8 + T) * 20480 + hg * 2560 + e * 16) = v;
      }
    }
  }
  STAMP(6);
}

constexpr int K2_WST = 25600;
constexpr int K2_PHI = 5 * K2_WST;
constexpr int K2_KV = K2_PHI + 20480;
constexpr int K2_FLG = K2_KV + 4096;
constexpr int K2_LDS = K2_FLG + 128;
constexpr int F_FULL = 0, F_FREE = 32, F_KV = 64, F_GO = 72, F_PUB = 76, F_RED = 80;

DI void lds_signal(unsigned addr, int lane) {
  if (lane == 0) asm volatile("ds_add_u32 %0, %1" :: "v"(addr), "v"(1u) : "memory");
}
DI void lds_wait(unsigned addr, unsigned want) {
  for (unsigned spins = 0; spins < (1u << 24); ++spins) {
    unsigned v;
    asm volatile("ds_read_b32 %0, %1\n\ts_waitcnt lgkmcnt(0)" : "=v"(v) : "v"(addr) : "memory");
    if ((unsigned)__builtin_amdgcn_readfirstlane(v) >= want) break;
    __builtin_amdgcn_s_sleep(1);
  }
}

template <int S0, int NB>
DI void k2_loadA(unsigned wa0, s16x8 (&A)[NB][5], int n) {
  const int s = S0 + n / 5, ks = n % 5;
#pragma unroll
  for (int m = 0; m < 5; ++m) {
    A[n % NB][m] = ldsr128(wa0 + (s % 5) * K2_WST + ks * 5120 + m * 1024);
  }
}
DI void k2_gemm_b(unsigned wa0, unsigned fb, int lane, const u32x4 (&Bf)[20], f32x4 (&acc)[5]) {
  constexpr int NB = 3;
  s16x8 A[NB][5];
  lds_wait(fb + F_FULL + 0, 4);
  k2_loadA<0, NB>(wa0, A, 0);
  k2_loadA<0, NB>(wa0, A, 1);
#pragma unroll
  for (int n = 0; n < 20; ++n) {
    if (n + 2 < 20) {
      if ((n + 2) % 5 == 0) {
        lds_wait(fb + F_FULL + 4 * ((n + 2) / 5), 4);
        lds_signal(fb + F_FREE + 4 * ((n + 2) / 5 - 1), lane);
      }
      k2_loadA<0, NB>(wa0, A, n + 2);
    }
    SBAR();
#pragma unroll
    for (int m = 0; m < 5; ++m) acc[m] = mfma32(A[n % NB][m], __builtin_bit_cast(s16x8, Bf[n]), acc[m]);
    SBAR();
  }
}
template <int S0>
DI void k2_gemm_a(unsigned wa0, s16x8 (&A)[4][5], const u32x4 (&Bf)[10], f32x4 (&acc)[5]) {
#pragma unroll
  for (int n = 0; n < 10; ++n) {
#pragma unroll
    for (int m = 0; m < 5; ++m) acc[m] = mfma32(A[n % 4][m], __builtin_bit_cast(s16x8, Bf[n]), acc[m]);
    SBAR();
    if (n + 4 < 10) k2_loadA<S0, 4>(wa0, A, n + 4);
    SBAR();
  }
}

__global__ void __launch_bounds__(768, 3)
k2_kernel(char* __restrict__ ws, char* __restrict__ out) {
  extern __shared__ __attribute__((aligned(16))) char smem[];
  const unsigned sbase = (unsigned)(size_t)(LAS char*)smem;
  const int tid = threadIdx.x, lane = tid & 63;
  const int w = __builtin_amdgcn_readfirstlane(tid >> 6);
  const int b = blockIdx.x, xcd = b & 7, idx = b >> 3;
  const int rt2 = (xcd >> 1) * 8 + (idx & 7);
  const int ct = (xcd & 1) * 4 + (idx >> 3);
  const int g = lane >> 4, c = lane & 15, qq = (lane >> 2) & 3, pp = lane & 3;
  const int hp = xcd * 2 + (idx >> 4), tg = idx & 15;
  GAS unsigned* cnt = (GAS unsigned*)(ws + WS_CNT);
  const unsigned fb = sbase + K2_FLG;
  const unsigned wa0 = sbase + lane * 16;
  STAMP2(0);

  if (w >= 8) {
    const int lw = w - 8;
    const char* Wp = ws + WS_WO + (size_t)ct * 204800;
    const unsigned woff = (unsigned)(lw * 1024 + lane * 16);
    auto issueW = [&](int s) {
      const unsigned lb = sbase + (s % 5) * K2_WST + lw * 1024;
#pragma unroll
      for (int i = 0; i < 6; ++i) glds16(Wp + s * K2_WST, woff + i * 4096, lb + i * 4096);
      if (lw == 0) glds16(Wp + s * K2_WST, woff + 24576, lb + 24576);
    };
    if (w == 8 && lane < 32) asm volatile("ds_write_b32 %0, %1\n\ts_waitcnt lgkmcnt(0)" :: "v"(fb + lane * 4), "v"(0u) : "memory");
    BAR();
    STAMP2(1);
    issueW(0); issueW(1); issueW(2);
    WAIT_V(12); lds_signal(fb + F_FULL + 0, lane);
    STAMP2(2);
    issueW(3);
    WAIT_V(12); lds_signal(fb + F_FULL + 4, lane);
    issueW(4);
    WAIT_V(12); lds_signal(fb + F_FULL + 8, lane);
    WAIT_V(6); lds_signal(fb + F_FULL + 12, lane);
    lds_wait(fb + F_FREE + 0, 4);
    issueW(5);
    WAIT_V(6); lds_signal(fb + F_FULL + 16, lane);
    lds_wait(fb + F_FREE + 4, 4);
    issueW(6);
    lds_wait(fb + F_FREE + 8, 4);
    issueW(7);
    WAIT_V(12); lds_signal(fb + F_FULL + 20, lane);
    WAIT_V(6); lds_signal(fb + F_FULL + 24, lane);
    WAIT_V(0); lds_signal(fb + F_FULL + 28, lane);
    STAMP2(4);
    STAMP2(7);
    return;
  }

  if (w >= 4) {
    __builtin_amdgcn_s_setprio(3);
    const int j = w - 4;
    const u32x2* pqp = (const u32x2*)(ws + WS_PQ + ((size_t)(hp * 2 + (lane >> 5)) * 2048 + tg * 128 + 32 * j + (lane & 31)) * 40);
    u32x2 pv[5];
#pragma unroll
    for (int i = 0; i < 5; ++i) pv[i] = pqp[i];
    SBAR();
    BAR();
    SBAR();
    const unsigned pb = sbase + K2_PHI + j * 5120;
    {
      float pk[20];
#pragma unroll
      for (int i = 0; i < 5; ++i) {
        pk[4 * i] = bflo(pv[i][0]); pk[4 * i + 1] = bfhi(pv[i][0]);
        pk[4 * i + 2] = bflo(pv[i][1]); pk[4 * i + 3] = bfhi(pv[i][1]);
      }
      unsigned ph[20];
      dual_softmax(pk, ph);
#pragma unroll
      for (int i = 0; i < 5; ++i) ldsw128(pb + lane * 80 + 16 * i, (u32x4){ph[4 * i], ph[4 * i + 1], ph[4 * i + 2], ph[4 * i + 3]});
    }
    WAIT_L0();
    s16x8 PB0[2][2];
    s16x4 PB1[2][2];
#pragma unroll
    for (int hh = 0; hh < 2; ++hh)
#pragma unroll
      for (int tt = 0; tt < 2; ++tt) {
        const unsigned pa = pb + (hh * 32 + tt * 16 + c) * 80;
        PB0[hh][tt] = ldsr128(pa + g * 16);
        s16x4 t1 = ldsr64(pa + 64 + (g & 1) * 8);
        const s16x4 z = {0, 0, 0, 0};
        PB1[hh][tt] = g < 2 ? t1 : z;
      }
    STAMP2(1);
    lds_wait(fb + F_KV, 4);
    STAMP2(2);
    const int r1 = c < 4 ? 16 + c : 20;
    const f32x4 z4 = (f32x4){0.f, 0.f, 0.f, 0.f};
#pragma unroll
    for (int hh = 0; hh < 2; ++hh) {
      const unsigned kb = sbase + K2_KV + hh * 21 * 96;
      s16x8 KA00 = ldsr128(kb + c * 96 + g * 16);
      s16x4 KA01 = ldsr64(kb + c * 96 + 64 + (g & 1) * 8);
      s16x8 KA10 = ldsr128(kb + r1 * 96 + g * 16);
      s16x4 KA11 = ldsr64(kb + r1 * 96 + 64 + (g & 1) * 8);
#pragma unroll
      for (int tt = 0; tt < 2; ++tt) {
        f32x4 n0 = mfma32(KA00, PB0[hh][tt], z4);
        n0 = mfma16(KA01, PB1[hh][tt], n0);
        f32x4 n1 = mfma32(KA10, PB0[hh][tt], z4);
        n1 = mfma16(KA11, PB1[hh][tt], n1);
        auto sw = __builtin_amdgcn_permlane16_swap(__float_as_uint(n1[0]), __float_as_uint(n1[0]), false, false);
        const float den = __uint_as_float(sw[1]);
        const float rd = rcp(den + 1e-6f);
        const int e0 = hh * 20 + 4 * g, e1 = hh * 20 + 16;
        const unsigned oa = pb + tt * 1280 + c * 16;
        ldsw64(oa + (e0 >> 3) * 256 + (e0 & 7) * 2, (u32x2){pack2(n0[0] * rd, n0[1] * rd), pack2(n0[2] * rd, n0[3] * rd)});
        if (g == 0) ldsw64(oa + (e1 >> 3) * 256 + (e1 & 7) * 2, (u32x2){pack2(n1[0] * rd, n1[1] * rd), pack2(n1[2] * rd, n1[3] * rd)});
      }
    }
    WAIT_L0();
    {
      const __amdgpu_buffer_rsrc_t rs = __builtin_amdgcn_make_buffer_rsrc(ws + WS_OA, 0, 2621440, 0x00020000);
      const unsigned gb = (unsigned)((tg * 8 + j * 2) * 20480 + hp * 1280);
#pragma unroll
      for (int i = 0; i < 3; ++i) {
        const int e = i * 64 + lane;
        if (e < 160) {
          const u32x4 v = *(LAS u32x4*)(pb + e * 16);
          __builtin_amdgcn_raw_buffer_store_b128(v, rs, gb + (e >= 80 ? 20480 - 1280 : 0) + e * 16, 0, 16);
        }
      }
    }
    STAMP2(3);
    WAIT_V(0);
    STAMP2(4);
    if (lane == 0) {
      unsigned old;
      asm volatile("ds_add_rtn_u32 %0, %1, %2\n\ts_waitcnt lgkmcnt(0)" : "=v"(old) : "v"(fb + F_PUB), "v"(1u) : "memory");
      if (old == 3u) __hip_atomic_fetch_add(cnt + tg * 64, 1u, __ATOMIC_RELAXED, __HIP_MEMORY_SCOPE_AGENT);
    }
    __builtin_amdgcn_s_setprio(0);
    {
      const __amdgpu_buffer_rsrc_t rsa = __builtin_amdgcn_make_buffer_rsrc(ws + WS_OA, 0, 2621440, 0x00020000);
      const unsigned fo = (unsigned)((rt2 * 4 + j) * 20480 + lane * 16);
      f32x4 acc[5];
#pragma unroll
      for (int m = 0; m < 5; ++m) acc[m] = (f32x4){0.f, 0.f, 0.f, 0.f};
      s16x8 A[4][5];
      lds_wait(fb + F_FULL + 24, 4);
      lds_wait(fb + F_FULL + 28, 4);
#pragma unroll
      for (int n = 0; n < 4; ++n) k2_loadA<6, 4>(wa0, A, n);
      SBAR();
      if (j == 0) {
        GAS unsigned* cp = cnt + (rt2 >> 1) * 64;
        unsigned spins = 0;
        while ((unsigned)__builtin_amdgcn_readfirstlane(__hip_atomic_load(cp, __ATOMIC_RELAXED, __HIP_MEMORY_SCOPE_AGENT)) < 16u) {
          __builtin_amdgcn_s_sleep(2);
          if (++spins > (1u << 22)) break;
        }
        asm volatile("" ::: "memory");
        lds_signal(fb + F_GO, lane);
      } else {
        lds_wait(fb + F_GO, 1);
      }
      STAMP2(5);
      u32x4 Bf[10];
#pragma unroll
      for (int ks = 0; ks < 10; ++ks) Bf[ks] = __builtin_amdgcn_raw_buffer_load_b128(rsa, fo + (10 + ks) * 1024, 0, 16);
      SBAR();
      k2_gemm_a<6>(wa0, A, Bf, acc);
#pragma unroll
      for (int m = 0; m < 5; ++m) *(LAS f32x4*)(pb + m * 1024 + lane * 16) = acc[m];
      WAIT_L0();
      lds_signal(fb + F_RED + 4 * j, lane);
    }
    STAMP2(7);
    return;
  }

  const unsigned fo = (unsigned)((rt2 * 4 + w) * 20480 + lane * 16);
  u32x4 Bf[20];
  {
    const int t = tid < 210 ? tid : 209;
    const char* kp = ws + WS_KVP + hp * 3360;
    const unsigned ko = (unsigned)t * 16u;
    u32x4 v[16];
#pragma unroll
    for (int r = 0; r < 16; ++r) v[r] = *(const u32x4*)(kp + r * 53760 + ko);
    SBAR();
    BAR();
    SBAR();
    float a[8];
#pragma unroll
    for (int jj = 0; jj < 8; ++jj) a[jj] = 0.f;
#pragma unroll
    for (int r = 0; r < 16; ++r)
#pragma unroll
      for (int jj = 0; jj < 4; ++jj) { a[2 * jj] += bflo(v[r][jj]); a[2 * jj + 1] += bfhi(v[r][jj]); }
    const int row = (t * 52429) >> 18, f8 = t - row * 5;
    ldsw128(sbase + K2_KV + row * 96 + f8 * 16, (u32x4){pack2(a[0], a[1]), pack2(a[2], a[3]), pack2(a[4], a[5]), pack2(a[6], a[7])});
    WAIT_L0();
    lds_signal(fb + F_KV, lane);
    SBAR();
#pragma unroll
    for (int ks = 0; ks < 20; ++ks) Bf[ks] = *(const u32x4*)(ws + WS_OB + fo + ks * 1024);
    SBAR();
  }
  STAMP2(1);
  const int tok = rt2 * 64 + 16 * w + c;
  const float gt = *(const float*)(ws + WS_GATE + tok * 4);
  f32x4 acc[2][5];
#pragma unroll
  for (int hh = 0; hh < 2; ++hh)
#pragma unroll
    for (int m = 0; m < 5; ++m) acc[hh][m] = (f32x4){0.f, 0.f, 0.f, 0.f};
  k2_gemm_b(wa0, fb, lane, Bf, acc[0]);
  STAMP2(3);
  {
    const __amdgpu_buffer_rsrc_t rsa = __builtin_amdgcn_make_buffer_rsrc(ws + WS_OA, 0, 2621440, 0x00020000);
    s16x8 A[4][5];
    lds_wait(fb + F_FULL + 16, 4);
    lds_wait(fb + F_FULL + 20, 4);
#pragma unroll
    for (int n = 0; n < 4; ++n) k2_loadA<4, 4>(wa0, A, n);
    SBAR();
    lds_wait(fb + F_GO, 1);
    STAMP2(4);
    u32x4 Ba[10];
#pragma unroll
    for (int ks = 0; ks < 10; ++ks) Ba[ks] = __builtin_amdgcn_raw_buffer_load_b128(rsa, fo + ks * 1024, 0, 16);
    SBAR();
    k2_gemm_a<4>(wa0, A, Ba, acc[1]);
    STAMP2(5);
    lds_wait(fb + F_RED + 4 * w, 1);
#pragma unroll
    for (int m = 0; m < 5; ++m) acc[1][m] += *(LAS f32x4*)(sbase + K2_PHI + w * 5120 + m * 1024 + lane * 16);
  }
  STAMP2(6);
  {
    const float g1 = 1.f - gt;
    char* op = out + ((size_t)tok * 640 + ct * 80 + 4 * g) * 2;
#pragma unroll
    for (int m = 0; m < 5; ++m) {
      float o[4];
#pragma unroll
      for (int r = 0; r < 4; ++r) o[r] = gt * acc[1][m][r] + g1 * acc[0][m][r];
      *(u32x2*)(op + m * 32) = (u32x2){pack2(o[0], o[1]), pack2(o[2], o[3])};
    }
  }
  STAMP2(7);
}

extern "C" void kernel_launch(void* const* d_in, const int* in_sizes, int n_in, void* d_out, int out_size, void* d_ws,
                              size_t ws_size, hipStream_t stream) {
  const char* x = (const char*)d_in[0];
  const char* wA = (const char*)d_in[1];
  const char* wphi = (const char*)d_in[2];
  const char* woA = (const char*)d_in[3];
  const char* wB = (const char*)d_in[5];
  const char* woB = (const char*)d_in[6];
  const char* gw = (const char*)d_in[7];
  (void)hipFuncSetAttribute((const void*)k1_kernel, hipFuncAttributeMaxDynamicSharedMemorySize, K1_LDS);
  (void)hipFuncSetAttribute((const void*)k2_kernel, hipFuncAttributeMaxDynamicSharedMemorySize, K2_LDS);
  k1_kernel<<<256, 512, K1_LDS, stream>>>(x, wA, wB, wphi, gw, woA, woB, (char*)d_ws);
  k2_kernel<<<256, 768, K2_LDS, stream>>>((char*)d_ws, (char*)d_out);
}
```

```cpp
#include <hip/hip_runtime.h>
#include <hip/hip_bf16.h>
#include <stdint.h>
#define SC1_MODE 1

typedef __attribute__((ext_vector_type(4))) short s16x4;
typedef __attribute__((ext_vector_type(8))) short s16x8;
typedef __attribute__((ext_vector_type(8))) __bf16 bf16x8;
typedef __attribute__((ext_vector_type(2))) __bf16 bf16x2;
typedef __attribute__((ext_vector_type(2))) float f32x2;
typedef __attribute__((ext_vector_type(4))) float f32x4;
typedef __attribute__((ext_vector_type(2))) unsigned u32x2;
typedef __attribute__((ext_vector_type(4))) unsigned u32x4;
#define LAS __attribute__((address_space(3)))
#define GAS __attribute__((address_space(1)))
#define DI __device__ __forceinline__

constexpr size_t WS_CNT = 0;
constexpr size_t WS_GATE = 4096;
constexpr size_t WS_PQ = 16384;
constexpr size_t WS_OB = WS_PQ + 2621440;
constexpr size_t WS_OA = WS_OB + 2621440;
constexpr size_t WS_KVP = WS_OA + 2621440;
constexpr size_t WS_WO = WS_KVP + 860160;

constexpr float LOG2E = 1.4426950408889634f;

DI unsigned pack2(float a, float b) {
  f32x2 v = {a, b};
  bf16x2 r = __builtin_convertvector(v, bf16x2);
  return __builtin_bit_cast(unsigned, r);
}
DI float rb(float a) { return (float)(__bf16)a; }
DI float bflo(unsigned u) { return __uint_as_float(u << 16); }
DI float bfhi(unsigned u) { return __uint_as_float(u & 0xffff0000u); }
DI float ex2(float a) { return __builtin_amdgcn_exp2f(a); }
DI float rcp(float a) { return __builtin_amdgcn_rcpf(a); }
DI float dot2bf(unsigned a, unsigned b, float c) {
  return __builtin_amdgcn_fdot2_f32_bf16(__builtin_bit_cast(bf16x2, a), __builtin_bit_cast(bf16x2, b), c, false);
}

DI s16x4 trr(unsigned addr) { return __builtin_amdgcn_ds_read_tr16_b64_v4i16((LAS s16x4*)addr); }
DI s16x8 cat8(s16x4 a, s16x4 b) { return __builtin_shufflevector(a, b, 0, 1, 2, 3, 4, 5, 6, 7); }
DI s16x8 ldsr128(unsigned addr) { return *(LAS s16x8*)addr; }
DI s16x4 ldsr64(unsigned addr) { return *(LAS s16x4*)addr; }
DI void ldsw128(unsigned addr, u32x4 v) { *(LAS u32x4*)addr = v; }
DI void ldsw64(unsigned addr, u32x2 v) { *(LAS u32x2*)addr = v; }
DI f32x4 mfma32(s16x8 a, s16x8 b, f32x4 c) {
  return __builtin_amdgcn_mfma_f32_16x16x32_bf16(__builtin_bit_cast(bf16x8, a), __builtin_bit_cast(bf16x8, b), c, 0, 0, 0);
}
DI f32x4 mfma16(s16x4 a, s16x4 b, f32x4 c) {
  const s16x4 z = {0, 0, 0, 0};
  return mfma32(cat8(a, z), cat8(b, z), c);
}
DI s16x8 mk8(u32x2 a, u32x2 b) {
  u32x4 v = {a[0], a[1], b[0], b[1]};
  return __builtin_bit_cast(s16x8, v);
}
DI void glds16(const char* base, unsigned off, unsigned lds) {
  __builtin_amdgcn_global_load_lds(base + off, (LAS void*)lds, 16, 0, 0);
}
DI float xmax4(float v) {
  auto a = __builtin_amdgcn_permlane16_swap(__float_as_uint(v), __float_as_uint(v), false, false);
  v = fmaxf(__uint_as_float(a[0]), __uint_as_float(a[1]));
  auto b = __builtin_amdgcn_permlane32_swap(__float_as_uint(v), __float_as_uint(v), false, false);
  return fmaxf(__uint_as_float(b[0]), __uint_as_float(b[1]));
}
DI float xsum4(float v) {
  auto a = __builtin_amdgcn_permlane16_swap(__float_as_uint(v), __float_as_uint(v), false, false);
  v = __uint_as_float(a[0]) + __uint_as_float(a[1]);
  auto b = __builtin_amdgcn_permlane32_swap(__float_as_uint(v), __float_as_uint(v), false, false);
  return __uint_as_float(b[0]) + __uint_as_float(b[1]);
}
#define WAIT_V(n) asm volatile("s_waitcnt vmcnt(" #n ")" ::: "memory")
#define WAIT_L0() do { asm volatile("" ::: "memory"); __builtin_amdgcn_s_waitcnt(0xc07f); asm volatile("" ::: "memory"); } while (0)
#define BAR() do { asm volatile("" ::: "memory"); __builtin_amdgcn_s_barrier(); asm volatile("" ::: "memory"); } while (0)
#define SBAR() __builtin_amdgcn_sched_barrier(0)
#define SGB(mask, n) __builtin_amdgcn_sched_group_barrier(mask, n, 0)

#define STAMP(i)
#define STAMP2(i)

#define ST_AUX 16
DI __amdgpu_buffer_rsrc_t ws_rsrc(char* ws) { return __builtin_amdgcn_make_buffer_rsrc(ws, 0, 16 << 20, 0x00020000); }
DI void st128(__amdgpu_buffer_rsrc_t r, unsigned off, u32x4 v) { __builtin_amdgcn_raw_buffer_store_b128(v, r, off, 0, ST_AUX); }
DI void st64(__amdgpu_buffer_rsrc_t r, unsigned off, u32x2 v) { __builtin_amdgcn_raw_buffer_store_b64(v, r, off, 0, ST_AUX); }

DI void dual_softmax(const float (&p)[20], unsigned (&ph)[20]) {
  float mx = p[0], mn = p[0];
#pragma unroll
  for (int i = 1; i < 20; ++i) { mx = fmaxf(mx, p[i]); mn = fminf(mn, p[i]); }
  const float bp = -mx * LOG2E, bn = mn * LOG2E;
  float ep[20], en[20], sp = 0.f, sn = 0.f;
#pragma unroll
  for (int i = 0; i < 20; ++i) {
    ep[i] = ex2(__builtin_fmaf(p[i], LOG2E, bp));
    en[i] = ex2(__builtin_fmaf(p[i], -LOG2E, bn));
    sp += ep[i];
    sn += en[i];
  }
  const float rp = rcp(sp), rn = rcp(sn);
#pragma unroll
  for (int i = 0; i < 10; ++i) {
    ph[i] = pack2(ep[2 * i] * rp, ep[2 * i + 1] * rp);
    ph[10 + i] = pack2(en[2 * i] * rn, en[2 * i + 1] * rn);
  }
}

constexpr int K1_WH = 15360;
constexpr int K1_XB = 6 * K1_WH;
constexpr int K1_XST = 18432;
constexpr int K1_WPHI = K1_XB + 3 * K1_XST;
constexpr int K1_ZERO = K1_WPHI + 4096;
constexpr int K1_RP = K1_ZERO + 1024;
constexpr int K1_LDS = K1_RP + 10240;
constexpr int K1_OST = 98304;
constexpr int K1_QK = 81920;

constexpr int ftoff(int ft) { return (ft / 5) * 160 + (ft % 5) * 8; }

template <int FH, int HM>
DI void k1_consumer_main(unsigned sbase, int th, int lane, f32x4 (&acc)[8][4], f32x4 (&acch)[8], char* ws) {
  constexpr int NT = FH ? 7 : 8;
  const int g = lane >> 4, c = lane & 15, qq = (lane >> 2) & 3, pp = lane & 3;
  const unsigned wa0 = sbase + (16 * (g >> 1) + 4 * (g & 1) + qq) * 480 + pp * 40;
  unsigned xh0[2], xb0[2];
#pragma unroll
  for (int a = 0; a < 2; ++a) {
    xh0[a] = sbase + K1_XB + c * 128 + (((4 * a + g) ^ (c & 7)) * 16);
    xb0[a] = xh0[a] + (16 + 64 * th) * 128;
  }
#pragma unroll
  for (int m = 0; m < 8; ++m) {
#pragma unroll
    for (int t = 0; t < 4; ++t) acc[m][t] = (f32x4){0.f, 0.f, 0.f, 0.f};
    acch[m] = (f32x4){0.f, 0.f, 0.f, 0.f};
  }
  s16x8 A[2][4], B[2][4], BH[2];
  auto loadA = [&](int grp, unsigned woff) {
#pragma unroll
    for (int m = 0; m < 4; ++m)
      if (4 * grp + m < NT) {
        const unsigned ad = wa0 + woff + ftoff(8 * FH + 4 * grp + m);
        A[grp][m] = cat8(trr(ad), trr(ad + 3840));
      }
  };
  auto loadB = [&](int buf, unsigned xoff) {
#pragma unroll
    for (int t = 0; t < 4; ++t) B[buf][t] = ldsr128(xb0[buf] + xoff + t * 2048);
    if (HM) BH[buf] = ldsr128(xh0[buf] + xoff);
  };
  auto mm = [&](int grp, int buf) {
#pragma unroll
    for (int m = 0; m < 4; ++m)
      if (4 * grp + m < NT) {
#pragma unroll
        for (int t = 0; t < 4; ++t) acc[4 * grp + m][t] = mfma32(A[grp][m], B[buf][t], acc[4 * grp + m][t]);
        if ((HM >> (4 * grp + m)) & 1) acch[4 * grp + m] = mfma32(A[grp][m], BH[buf], acch[4 * grp + m]);
      }
  };
  auto kstep = [&](int a, unsigned woff, unsigned nwoff, unsigned nxoff, bool last) {
    loadA(1, woff);
    mm(0, a);
    SGB(0x8, 1); SGB(0x100, 2); SGB(0x8, 1); SGB(0x100, 2); SGB(0x8, 1); SGB(0x100, 2); SGB(0x8, 1); SGB(0x100, 2);
    SBAR();
    WAIT_L0();
    BAR();
    if (!last) {
      loadB(a ^ 1, nxoff);
      loadA(0, nwoff);
    }
    mm(1, a);
    if (!last) {
      SGB(0x8, 1); SGB(0x100, 2); SGB(0x8, 1); SGB(0x100, 2); SGB(0x8, 1); SGB(0x100, 2); SGB(0x8, 1); SGB(0x100, 2);
      SGB(0x8, 1); SGB(0x100, 2); SGB(0x8, 1); SGB(0x100, 2); SGB(0x8, 1); SGB(0x100, 1);
    }
    SBAR();
  };
  STAMP(1);
  BAR();
  STAMP(2);
  loadB(0, 0);
  loadA(0, 0);
  SBAR();
  unsigned woff = 0, xoff = 0;
#pragma unroll 1
  for (int s = 0; s < 9; ++s) {
    unsigned nw = woff + 2 * K1_WH, nx = xoff + K1_XST;
    if (nw >= 6 * K1_WH) { nw = 0; nx = 0; }
    kstep(0, woff, woff + K1_WH, xoff, false);
    kstep(1, woff + K1_WH, nw, nx, false);
    woff = nw; xoff = nx;
  }
  kstep(0, 0, K1_WH, 0, false);
  kstep(1, K1_WH, 0, 0, true);
}

template <int FH, int HM, bool ISB>
DI void k1_dump(unsigned sbase, int th, int lane, const f32x4 (&acc)[8][4], const f32x4 (&acch)[8]) {
  constexpr int NT = FH ? 7 : 8;
  const int g = lane >> 4, c = lane & 15;
#pragma unroll
  for (int t = 0; t < 4; ++t) {
    u32x2 v[8];
#pragma unroll
    for (int m = 0; m < NT; ++m) v[m] = (u32x2){pack2(acc[m][t][0], acc[m][t][1]), pack2(acc[m][t][2], acc[m][t][3])};
    const int tok = 64 * th + 16 * t + c;
    if (ISB) {
      const unsigned rowa = sbase + (g * 144 + 16 + tok) * 160;
      if (FH == 0) {
        ldsw128(rowa + 0, (u32x4){v[0][0], v[0][1], v[1][0], v[1][1]});
        ldsw128(rowa + 16, (u32x4){v[2][0], v[2][1], v[3][0], v[3][1]});
        ldsw128(rowa + 32, (u32x4){v[4][0], v[4][1], 0u, 0u});
        ldsw128(rowa + 48, (u32x4){0u, 0u, 0u, 0u});
        ldsw128(rowa + 64, (u32x4){v[5][0], v[5][1], v[6][0], v[6][1]});
        ldsw64(rowa + 80, v[7]);
      } else {
        ldsw64(rowa + 88, v[0]);
        ldsw128(rowa + 96, (u32x4){v[1][0], v[1][1], v[2][0], v[2][1]});
        ldsw128(rowa + 112, (u32x4){v[3][0], v[3][1], v[4][0], v[4][1]});
        ldsw128(rowa + 128, (u32x4){v[5][0], v[5][1], v[6][0], v[6][1]});
      }
    } else {
      const unsigned qa = sbase + K1_QK + (4 * th + t) * 5120 + lane * 16;
      if (FH == 0) {
#pragma unroll
        for (int i = 0; i < 4; ++i) ldsw128(qa + i * 1024, (u32x4){v[2 * i][0], v[2 * i][1], v[2 * i + 1][0], v[2 * i + 1][1]});
      } else {
        ldsw128(qa + 4096, (u32x4){v[0][0], v[0][1], v[1][0], v[1][1]});
        const unsigned rowa = sbase + (g * 128 + tok) * 160;
        ldsw128(rowa + 80, (u32x4){v[2][0], v[2][1], v[3][0], v[3][1]});
        ldsw128(rowa + 96, (u32x4){v[4][0], v[4][1], v[5][0], v[5][1]});
        ldsw128(rowa + 112, (u32x4){v[6][0], v[6][1], 0x00003f80u, 0u});
      }
    }
  }
  if (ISB) {
#pragma unroll
    for (int m = 0; m < NT; ++m)
      if ((HM >> m) & 1)
        ldsw64(sbase + (g * 144 + c) * 160 + 64 + (8 * FH + m - 5) * 8, (u32x2){pack2(acch[m][0], acch[m][1]), pack2(acch[m][2], acch[m][3])});
  }
}

template <int FH, int HM, bool ISB>
DI void k1_consumer(unsigned sbase, int th, int lane, char* ws) {
  f32x4 acc[8][4], acch[8];
  k1_consumer_main<FH, HM>(sbase, th, lane, acc, acch, ws);
  STAMP(3);
  k1_dump<FH, HM, ISB>(sbase, th, lane, acc, acch);
}

__global__ void __launch_bounds__(512, 2)
k1_kernel(const char* __restrict__ x, const char* __restrict__ wA, const char* __restrict__ wB,
          const char* __restrict__ wphi, const char* __restrict__ gatew, const char* __restrict__ woA,
          const char* __restrict__ woB, char* __restrict__ ws) {
  extern __shared__ __attribute__((aligned(16))) char smem[];
  const unsigned sbase = (unsigned)(size_t)(LAS char*)smem;
  const int tid = threadIdx.x, lane = tid & 63;
  const int w = __builtin_amdgcn_readfirstlane(tid >> 6);
  const int b = blockIdx.x, xcd = b & 7, idx = b >> 3;
  const int rt = (xcd & 1) * 8 + (idx & 7);
  const int cg = (xcd >> 1) * 4 + (idx >> 3);
  const bool isB = cg >= 8;
  const int hg = cg & 7;
  const int row0 = rt * 128;
  const int g = lane >> 4, c = lane & 15, qq = (lane >> 2) & 3, pp = lane & 3;
  STAMP(0);
  if (w == 7) {
    unsigned pf[5];
    {
      const int l2 = 64 + lane;
      pf[0] = *(const unsigned*)(x + (size_t)(b * 80 + lane) * 128);
      pf[1] = *(const unsigned*)(wA + (size_t)(b * 75 + lane) * 128);
      pf[2] = *(const unsigned*)(wB + (size_t)(b * 75 + lane) * 128);
      const char* p3 = lane < 16 ? x + (size_t)(b * 80 + l2) * 128
                     : (lane < 27 ? wA + (size_t)(b * 75 + 48 + lane) * 128 : wB + (size_t)(b * 75 + 37 + (lane < 38 ? lane : 37)) * 128);
      pf[3] = *(const unsigned*)p3;
      const char* p4 = lane < 25 ? woA + (size_t)(b * 25 + lane) * 128 : woB + (size_t)(b * 25 + (lane < 50 ? lane - 25 : 24)) * 128;
      pf[4] = *(const unsigned*)p4;
    }
    SBAR();
    BAR(); BAR(); BAR(); BAR();
#pragma unroll
    for (int i = 0; i < 4; ++i) {
      int e = i * 64 + lane;
      e = e > 199 ? 199 : e;
      glds16(wphi + hg * 3200, (unsigned)e * 16u, sbase + K1_WPHI + i * 1024);
    }
#pragma unroll
    for (int r = 0; r < 2; ++r) {
      const int u = r == 0 ? b : (b < 64 ? 256 + b : b);
      const int uct = u / 40, uks = u - uct * 40;
      const char* wsrc = (uks < 20 ? woB + (size_t)uks * 40960 : woA + (size_t)(uks - 20) * 40960) + uct * 160;
#pragma unroll
      for (int i = 0; i < 5; ++i) {
        const int e = i * 64 + lane;
        const int rho = (e * 6554) >> 16, ch = e - rho * 10;
        const int kap = (rho & ~12) | ((rho & 4) << 1) | ((rho & 8) >> 1);
        glds16(wsrc, (unsigned)(kap * 1280 + ch * 16), sbase + K1_RP + r * 5120 + i * 1024);
      }
    }
    asm volatile("ds_write_b128 %0, %1" :: "v"(sbase + K1_ZERO + lane * 16), "v"((u32x4){0u, 0u, 0u, 0u}) : "memory");
    if (b == 0 && lane < 16) *(unsigned*)(ws + WS_CNT + lane * 256) = 0u;
    const int gtok = row0 + cg * 8 + (lane >> 3);
    u32x4 xv[10], gv[10];
#pragma unroll
    for (int i = 0; i < 10; ++i) {
      gv[i] = *(const u32x4*)(gatew + (lane & 7) * 160 + i * 16);
      xv[i] = *(const u32x4*)(x + (size_t)gtok * 1280 + (lane & 7) * 160 + i * 16);
    }
    SBAR();
#pragma unroll 1
    for (int i = 0; i < 7; ++i) BAR();
    SBAR();
    asm volatile("" :: "v"(pf[0]), "v"(pf[1]), "v"(pf[2]), "v"(pf[3]), "v"(pf[4]));
    {
      float a0 = 0.f, a1 = 0.f;
#pragma unroll
      for (int i = 0; i < 10; ++i)
#pragma unroll
        for (int j = 0; j < 4; j += 2) {
          a0 = dot2bf(xv[i][j], gv[i][j], a0);
          a1 = dot2bf(xv[i][j + 1], gv[i][j + 1], a1);
        }
      float a = a0 + a1;
      BAR();
      a += __int_as_float(__builtin_amdgcn_update_dpp(0, __float_as_int(a), 0xB1, 0xf, 0xf, true));
      a += __int_as_float(__builtin_amdgcn_update_dpp(0, __float_as_int(a), 0x4E, 0xf, 0xf, true));
      a += __int_as_float(__builtin_amdgcn_update_dpp(0, __float_as_int(a), 0x141, 0xf, 0xf, true));
      if ((lane & 7) == 0) *(float*)(ws + WS_GATE + gtok * 4) = rcp(1.f + ex2(-a * LOG2E));
      BAR();
    }
#pragma unroll
    for (int r = 0; r < 2; ++r) {
      const int u = r == 0 ? b : 256 + b;
      const unsigned ra = sbase + K1_RP + r * 5120 + (16 * (g >> 1) + 4 * (g & 1) + qq) * 160 + pp * 8;
      s16x8 F[5];
#pragma unroll
      for (int m = 0; m < 5; ++m) F[m] = cat8(trr(ra + m * 32), trr(ra + m * 32 + 1280));
      BAR();
      if (r == 0 || b < 64) {
#pragma unroll
        for (int m = 0; m < 5; ++m) {
          st128(ws_rsrc(ws), (unsigned)(WS_WO + u * 5120 + m * 1024 + lane * 16), __builtin_bit_cast(u32x4, F[m]));
        }
      }
      BAR();
    }
    BAR(); BAR();
    WAIT_L0();
    BAR(); BAR();
  } else if (w >= 4) {
    const int lw = w - 4;
    const char* Wb = (isB ? wB : wA) + hg * 160;
    const char* Xb = x;
    unsigned xoff[6], woff[5];
    {
      const int l8 = lane >> 3, slot = lane & 7;
      const unsigned sw = (unsigned)((slot ^ l8) * 16);
#pragma unroll
      for (int i = 0; i < 6; ++i) {
        int tok = row0 - 16 + (3 * i + lw) * 8 + l8;
        tok = tok < 0 ? 0 : tok;
        xoff[i] = (unsigned)tok * 1280u + sw;
      }
    }
    {
      const int u0 = lw * 64 + lane;
      int rho = (u0 * 2185) >> 16;
      int ch = u0 - rho * 30;
#pragma unroll
      for (int i = 0; i < 5; ++i) {
        const int kap = (rho & ~12) | ((rho & 4) << 1) | ((rho & 8) >> 1);
        const int part = (ch >= 10) + (ch >= 20);
        woff[i] = (unsigned)(kap * 3840 + part * (1280 - 160) + ch * 16);
        rho += 6; ch += 12;
        if (ch >= 30) { ch -= 30; rho += 1; }
      }
    }
    unsigned wl = sbase + lw * 1024, xl = sbase + K1_XB + lw * 1024;
    auto issueW = [&]() {
#pragma unroll
      for (int i = 0; i < 5; ++i) glds16(Wb, woff[i], wl + i * 3072);
      Wb += 32 * 3840;
      wl += K1_WH;
      if (wl >= sbase + K1_XB) wl -= K1_XB;
    };
    auto issueX = [&]() {
#pragma unroll
      for (int i = 0; i < 6; ++i) glds16(Xb, xoff[i], xl + i * 3072);
      Xb += 128;
      xl += K1_XST;
      if (xl >= sbase + K1_WPHI) xl -= 3 * K1_XST;
    };
    STAMP(1);
    issueX(); issueW(); issueW(); WAIT_V(5);
    STAMP(2);
    BAR();
    issueX(); issueW(); issueW(); WAIT_V(16); BAR();
    issueX(); issueW(); issueW(); WAIT_V(21); BAR();
    issueX(); issueW(); issueW(); WAIT_V(32); BAR();
#pragma unroll 1
    for (int i = 0; i < 6; ++i) {
      issueW(); WAIT_V(26); BAR();
      issueX(); issueW(); WAIT_V(32); BAR();
    }
    WAIT_V(21); BAR();
    WAIT_V(16); BAR();
    WAIT_V(5); BAR();
    WAIT_V(0); BAR();
    BAR();
  } else {
    const int th = w & 1;
    if (isB) {
      if (w == 0) k1_consumer<0, 0x60, true>(sbase, th, lane, ws);
      else if (w == 1) k1_consumer<0, 0x80, true>(sbase, th, lane, ws);
      else if (w == 2) k1_consumer<1, 0x0f, true>(sbase, th, lane, ws);
      else k1_consumer<1, 0x70, true>(sbase, th, lane, ws);
    } else {
      if (w < 2) k1_consumer<0, 0, false>(sbase, th, lane, ws);
      else k1_consumer<1, 0, false>(sbase, th, lane, ws);
    }
  }
  WAIT_L0();
  STAMP(4);
  BAR();
  STAMP(5);

  if (!isB) {
    {
      const int tok = 16 * w + c;
      u32x2 in[2][5];
      {
        const unsigned qa = sbase + K1_QK + w * 5120 + lane * 16;
#pragma unroll
        for (int i = 0; i < 5; ++i) {
          const u32x4 v = *(LAS u32x4*)(qa + i * 1024);
          in[(2 * i) / 5][(2 * i) % 5] = (u32x2){v[0], v[1]};
          in[(2 * i + 1) / 5][(2 * i + 1) % 5] = (u32x2){v[2], v[3]};
        }
      }
      f32x4 P[2][5];
      const unsigned wpa = (pp == g) ? (sbase + K1_WPHI + g * 800 + qq * 40) : (sbase + K1_ZERO);
#pragma unroll
      for (int mo = 0; mo < 5; ++mo) {
        const unsigned a0 = wpa + mo * 8;
        s16x8 W0 = cat8(trr(a0), trr(a0 + 160));
        s16x8 W1 = cat8(trr(a0 + 320), trr(a0 + 480));
        s16x4 W2 = trr(a0 + 640);
#pragma unroll
        for (int p = 1; p >= 0; --p) {
          f32x4 r = (f32x4){0.f, 0.f, 0.f, 0.f};
          r = mfma32(W0, mk8(in[p][0], in[p][1]), r);
          r = mfma32(W1, mk8(in[p][2], in[p][3]), r);
          r = mfma16(W2, __builtin_bit_cast(s16x4, in[p][4]), r);
          P[p][mo] = r;
        }
      }
      u32x2* pqp = (u32x2*)(ws + WS_PQ + ((size_t)(hg * 4 + g) * 2048 + row0 + tok) * 40);
#pragma unroll
      for (int m = 0; m < 5; ++m) pqp[m] = (u32x2){pack2(P[0][m][0], P[0][m][1]), pack2(P[0][m][2], P[0][m][3])};
      float pv[20];
#pragma unroll
      for (int m = 0; m < 5; ++m)
#pragma unroll
        for (int r = 0; r < 4; ++r) pv[4 * m + r] = P[1][m][r];
      unsigned ph[20];
      dual_softmax(pv, ph);
      const unsigned rowa = sbase + (g * 128 + tok) * 160;
#pragma unroll
      for (int i = 0; i < 5; ++i) ldsw128(rowa + 16 * i, (u32x4){ph[4 * i], ph[4 * i + 1], ph[4 * i + 2], ph[4 * i + 3]});
    }
    WAIT_L0();
    BAR();
    {
      const int hl = w >> 1, jt = w & 1;
      const unsigned rbase = sbase + (hl * 128 + 4 * g + qq) * 160 + pp * 8;
      f32x4 kvacc[3];
#pragma unroll
      for (int it = 0; it < 3; ++it) kvacc[it] = (f32x4){0.f, 0.f, 0.f, 0.f};
#pragma unroll
      for (int ks = 0; ks < 4; ++ks) {
        const unsigned r0 = rbase + ks * 32 * 160;
        s16x8 Bv = cat8(trr(r0 + 80 + jt * 32), trr(r0 + 16 * 160 + 80 + jt * 32));
#pragma unroll
        for (int it = 0; it < 3; ++it) {
          s16x8 Ap = cat8(trr(r0 + it * 32), trr(r0 + 16 * 160 + it * 32));
          kvacc[it] = mfma32(Ap, Bv, kvacc[it]);
        }
      }
      const int d = jt * 16 + c;
      if (d < 21) {
        char* kp = ws + WS_KVP + ((size_t)(rt * 32 + hg * 4 + hl) * 21 + d) * 80;
#pragma unroll
        for (int it = 0; it < 3; ++it) {
          const int f = it * 16 + 4 * g;
          if (f < 40) *(u32x2*)(kp + f * 2) = (u32x2){pack2(kvacc[it][0], kvacc[it][1]), pack2(kvacc[it][2], kvacc[it][3])};
        }
      }
    }
  } else {
    const int T = w;
    const bool par = (c & 1) != 0;
    const bool cur0 = c - (4 * g + (c & 1)) >= 0, cur1 = c - (4 * g + 2 + (c & 1)) >= 0;
    const bool first = (rt == 0 && T == 0);
    const bool ok0 = cur0 || !first, ok1 = cur1 || !first;
    const unsigned hm = par ? 0xffff0000u : 0x0000ffffu;
    const unsigned mC0 = cur0 ? hm : 0u, mP0 = cur0 ? 0u : hm, mC1 = cur1 ? hm : 0u, mP1 = cur1 ? 0u : hm;
    const unsigned r0 = sbase + (16 * T) * 160;
    const unsigned ka = r0 + c * 160 + 64 + g * 16;
    const unsigned va = r0 + (4 * g + qq) * 160 + 104 + pp * 8;
    const f32x4 z4 = (f32x4){0.f, 0.f, 0.f, 0.f};
    f32x4 s0[4], s1[4];
    s16x8 V0[4], V1[4];
#pragma unroll
    for (int h = 0; h < 4; ++h) {
      s16x8 Kp = ldsr128(ka + h * 23040);
      s16x8 Kc = ldsr128(ka + h * 23040 + 2560);
      s16x8 Q = ldsr128(ka + h * 23040 + 2560 - 64);
      s0[h] = mfma32(Kp, Q, z4);
      s1[h] = mfma32(Kc, Q, z4);
    }
#pragma unroll
    for (int h = 0; h < 4; ++h) {
      V0[h] = cat8(trr(va + h * 23040), trr(va + h * 23040 + 2560));
      V1[h] = cat8(trr(va + h * 23040 + 32), trr(va + h * 23040 + 2560 + 32));
    }
    constexpr float SC = 0.22360679775f * LOG2E;
    float ea[4], eb[4], mx[4], sum[4];
#pragma unroll
    for (int h = 0; h < 4; ++h) {
      const float p0 = par ? s0[h][1] : s0[h][0], p1 = par ? s0[h][3] : s0[h][2];
      const float c0 = par ? s1[h][1] : s1[h][0], c1 = par ? s1[h][3] : s1[h][2];
      float a = cur0 ? c0 : p0, bq = cur1 ? c1 : p1;
      a = ok0 ? a : -3.0e38f;
      bq = ok1 ? bq : -3.0e38f;
      ea[h] = a; eb[h] = bq;
      mx[h] = fmaxf(a, bq);
    }
#pragma unroll
    for (int h = 0; h < 4; ++h) mx[h] = xmax4(mx[h]);
#pragma unroll
    for (int h = 0; h < 4; ++h) {
      const float bb = -mx[h] * SC;
      ea[h] = ok0 ? ex2(__builtin_fmaf(ea[h], SC, bb)) : 0.f;
      eb[h] = ok1 ? ex2(__builtin_fmaf(eb[h], SC, bb)) : 0.f;
      sum[h] = ea[h] + eb[h];
    }
#pragma unroll
    for (int h = 0; h < 4; ++h) sum[h] = xsum4(sum[h]);
    const unsigned ost = sbase + K1_OST + w * 2560;
#pragma unroll
    for (int h = 0; h < 4; ++h) {
      const float rs = rcp(sum[h]);
      const float pa = ea[h] * rs, pbv = eb[h] * rs;
      const unsigned da = pack2(pa, pa), db = pack2(pbv, pbv);
      u32x4 pb = {da & mP0, db & mP1, da & mC0, db & mC1};
      s16x8 Pb = __builtin_bit_cast(s16x8, pb);
      f32x4 o0 = mfma32(V0[h], Pb, z4);
      f32x4 o1 = mfma32(V1[h], Pb, z4);
      const int e0 = h * 20 + 4 * g, e1 = h * 20 + 16;
      ldsw64(ost + (e0 >> 3) * 256 + c * 16 + (e0 & 7) * 2, (u32x2){pack2(o0[0], o0[1]), pack2(o0[2], o0[3])});
      if (g == 0) ldsw64(ost + (e1 >> 3) * 256 + c * 16 + (e1 & 7) * 2, (u32x2){pack2(o1[0], o1[1]), pack2(o1[2], o1[3])});
    }
    WAIT_L0();
#pragma unroll
    for (int i = 0; i < 3; ++i) {
      const int e = i * 64 + lane;
      if (e < 160) {
        u32x4 v = *(LAS u32x4*)(ost + e * 16);
        *(u32x4*)(ws + WS_OB + (size_t)(rt * 8 + T) * 20480 + hg * 2560 + e * 16) = v;
      }
    }
  }
  STAMP(6);
}

#define NB_EARLY 8
constexpr int K2_WST = 25600;
constexpr int K2_PHI = 5 * K2_WST;
constexpr int K2_RED = 2 * K2_WST;
constexpr int K2_KV = 6 * K2_WST;
constexpr int K2_FLG = K2_KV + 4096;
constexpr int K2_LDS = K2_FLG + 128;
constexpr int F_FULL = 0, F_FREE = 32, F_KV = 64, F_PA = 68, F_GO = 72, F_PUB = 76, F_RED = 80;

DI void lds_signal(unsigned addr, int lane) {
  if (lane == 0) asm volatile("ds_add_u32 %0, %1" :: "v"(addr), "v"(1u) : "memory");
}
DI void lds_wait(unsigned addr, unsigned want) {
  for (unsigned spins = 0; spins < (1u << 24); ++spins) {
    unsigned v;
    asm volatile("ds_read_b32 %0, %1\n\ts_waitcnt lgkmcnt(0)" : "=v"(v) : "v"(addr) : "memory");
    if ((unsigned)__builtin_amdgcn_readfirstlane(v) >= want) break;
    __builtin_amdgcn_s_sleep(1);
  }
}

template <int S0, int NB>
DI void k2_loadA(unsigned wa0, s16x8 (&A)[NB][5], int n) {
  const int s = S0 + n / 5, ks = n % 5;
#pragma unroll
  for (int m = 0; m < 5; ++m) {
    A[n % NB][m] = ldsr128(wa0 + (s % 6) * K2_WST + ks * 5120 + m * 1024);
  }
}
DI void k2_gemm_b(unsigned wa0, unsigned fb, int lane, const u32x4 (&Bf)[20], f32x4 (&acc)[5]) {
  constexpr int NB = 3;
  s16x8 A[NB][5];
  lds_wait(fb + F_FULL + 0, 2);
  k2_loadA<0, NB>(wa0, A, 0);
  k2_loadA<0, NB>(wa0, A, 1);
#pragma unroll
  for (int n = 0; n < 20; ++n) {
    if (n + 2 < 20) {
      if ((n + 2) % 5 == 0) {
        lds_wait(fb + F_FULL + 4 * ((n + 2) / 5), 2);
        lds_signal(fb + F_FREE + 4 * ((n + 2) / 5 - 1), lane);
      }
      k2_loadA<0, NB>(wa0, A, n + 2);
    }
    SBAR();
#pragma unroll
    for (int m = 0; m < 5; ++m) acc[m] = mfma32(A[n % NB][m], __builtin_bit_cast(s16x8, Bf[n]), acc[m]);
    SBAR();
  }
}
template <int S0>
DI void k2_gemm_a(unsigned wa0, s16x8 (&A)[4][5], const u32x4 (&Bf)[10], f32x4 (&acc)[5]) {
#pragma unroll
  for (int n = 0; n < 10; ++n) {
#pragma unroll
    for (int m = 0; m < 5; ++m) acc[m] = mfma32(A[n % 4][m], __builtin_bit_cast(s16x8, Bf[n]), acc[m]);
    SBAR();
    if (n + 4 < 10) k2_loadA<S0, 4>(wa0, A, n + 4);
    SBAR();
  }
}

__global__ void __launch_bounds__(640, 3)
k2_kernel(char* __restrict__ ws, char* __restrict__ out) {
  extern __shared__ __attribute__((aligned(16))) char smem[];
  const unsigned sbase = (unsigned)(size_t)(LAS char*)smem;
  const int tid = threadIdx.x, lane = tid & 63;
  const int w = __builtin_amdgcn_readfirstlane(tid >> 6);
  const int b = blockIdx.x, xcd = b & 7, idx = b >> 3;
  const int rt2 = (xcd >> 1) * 8 + (idx & 7);
  const int ct = (xcd & 1) * 4 + (idx >> 3);
  const int g = lane >> 4, c = lane & 15, qq = (lane >> 2) & 3, pp = lane & 3;
  const int hp = xcd * 2 + (idx >> 4), tg = idx & 15;
  GAS unsigned* cnt = (GAS unsigned*)(ws + WS_CNT);
  const unsigned fb = sbase + K2_FLG;
  const unsigned wa0 = sbase + lane * 16;
  STAMP2(0);

  if (w >= 8) {
    const int lw = w - 8;
    const char* Wp = ws + WS_WO + (size_t)ct * 204800;
    const unsigned woff = (unsigned)(lw * 1024 + lane * 16);
    auto issueW = [&](int s) {
      const unsigned lb = sbase + (s % 6) * K2_WST + lw * 1024;
#pragma unroll
      for (int i = 0; i < 12; ++i) glds16(Wp + s * K2_WST, woff + i * 2048, lb + i * 2048);
      if (lw == 0) glds16(Wp + s * K2_WST, woff + 24576, lb + 24576);
    };
    if (w == 8 && lane < 32) asm volatile("ds_write_b32 %0, %1\n\ts_waitcnt lgkmcnt(0)" :: "v"(fb + lane * 4), "v"(0u) : "memory");
    BAR();
    STAMP2(1);
    issueW(0); issueW(1); issueW(2);
    WAIT_V(24); lds_signal(fb + F_FULL + 0, lane);
    STAMP2(2);
    issueW(3);
    WAIT_V(24); lds_signal(fb + F_FULL + 4, lane);
    issueW(4);
    WAIT_V(24); lds_signal(fb + F_FULL + 8, lane);
    WAIT_V(12); lds_signal(fb + F_FULL + 12, lane);
    WAIT_V(0); lds_signal(fb + F_FULL + 16, lane);
    lds_wait(fb + F_FREE + 0, 4);
    issueW(6);
    lds_wait(fb + F_PA, 4);
    issueW(5);
    lds_wait(fb + F_FREE + 4, 4);
    issueW(7);
    WAIT_V(24); lds_signal(fb + F_FULL + 24, lane);
    WAIT_V(12); lds_signal(fb + F_FULL + 20, lane);
    WAIT_V(0); lds_signal(fb + F_FULL + 28, lane);
    STAMP2(4);
    STAMP2(7);
    return;
  }

  if (w >= 4) {
    __builtin_amdgcn_s_setprio(3);
    const int j = w - 4;
    const u32x2* pqp = (const u32x2*)(ws + WS_PQ + ((size_t)(hp * 2 + (lane >> 5)) * 2048 + tg * 128 + 32 * j + (lane & 31)) * 40);
    u32x2 pv[5];
#pragma unroll
    for (int i = 0; i < 5; ++i) pv[i] = pqp[i];
    float gta[2];
#pragma unroll
    for (int tt = 0; tt < 2; ++tt) gta[tt] = *(const float*)(ws + WS_GATE + (tg * 128 + 32 * j + 16 * tt + c) * 4);
    SBAR();
    BAR();
    SBAR();
    const unsigned pb = sbase + K2_PHI + j * 5120;
    {
      float pk[20];
#pragma unroll
      for (int i = 0; i < 5; ++i) {
        pk[4 * i] = bflo(pv[i][0]); pk[4 * i + 1] = bfhi(pv[i][0]);
        pk[4 * i + 2] = bflo(pv[i][1]); pk[4 * i + 3] = bfhi(pv[i][1]);
      }
      unsigned ph[20];
      dual_softmax(pk, ph);
#pragma unroll
      for (int i = 0; i < 5; ++i) ldsw128(pb + lane * 80 + 16 * i, (u32x4){ph[4 * i], ph[4 * i + 1], ph[4 * i + 2], ph[4 * i + 3]});
    }
    WAIT_L0();
    s16x8 PB0[2][2];
    s16x4 PB1[2][2];
#pragma unroll
    for (int hh = 0; hh < 2; ++hh)
#pragma unroll
      for (int tt = 0; tt < 2; ++tt) {
        const unsigned pa = pb + (hh * 32 + tt * 16 + c) * 80;
        PB0[hh][tt] = ldsr128(pa + g * 16);
        s16x4 t1 = ldsr64(pa + 64 + (g & 1) * 8);
        const s16x4 z = {0, 0, 0, 0};
        PB1[hh][tt] = g < 2 ? t1 : z;
      }
    STAMP2(1);
    lds_wait(fb + F_KV, 4);
    STAMP2(2);
    const int r1 = c < 4 ? 16 + c : 20;
    const f32x4 z4 = (f32x4){0.f, 0.f, 0.f, 0.f};
#pragma unroll
    for (int hh = 0; hh < 2; ++hh) {
      const unsigned kb = sbase + K2_KV + hh * 21 * 96;
      s16x8 KA00 = ldsr128(kb + c * 96 + g * 16);
      s16x4 KA01 = ldsr64(kb + c * 96 + 64 + (g & 1) * 8);
      s16x8 KA10 = ldsr128(kb + r1 * 96 + g * 16);
      s16x4 KA11 = ldsr64(kb + r1 * 96 + 64 + (g & 1) * 8);
#pragma unroll
      for (int tt = 0; tt < 2; ++tt) {
        f32x4 n0 = mfma32(KA00, PB0[hh][tt], z4);
        n0 = mfma16(KA01, PB1[hh][tt], n0);
        f32x4 n1 = mfma32(KA10, PB0[hh][tt], z4);
        n1 = mfma16(KA11, PB1[hh][tt], n1);
        auto sw = __builtin_amdgcn_permlane16_swap(__float_as_uint(n1[0]), __float_as_uint(n1[0]), false, false);
        const float den = __uint_as_float(sw[1]);
        const float rd = gta[tt] * rcp(den + 1e-6f);
        const int e0 = hh * 20 + 4 * g, e1 = hh * 20 + 16;
        const unsigned oa = pb + tt * 1280 + c * 16;
        ldsw64(oa + (e0 >> 3) * 256 + (e0 & 7) * 2, (u32x2){pack2(n0[0] * rd, n0[1] * rd), pack2(n0[2] * rd, n0[3] * rd)});
        if (g == 0) ldsw64(oa + (e1 >> 3) * 256 + (e1 & 7) * 2, (u32x2){pack2(n1[0] * rd, n1[1] * rd), pack2(n1[2] * rd, n1[3] * rd)});
      }
    }
    WAIT_L0();
    STAMP2(8);
    {
      const __amdgpu_buffer_rsrc_t rs = __builtin_amdgcn_make_buffer_rsrc(ws + WS_OA, 0, 2621440, 0x00020000);
      const unsigned gb = (unsigned)((tg * 8 + j * 2) * 20480 + hp * 1280);
      u32x4 v[3];
#pragma unroll
      for (int i = 0; i < 3; ++i) v[i] = *(LAS u32x4*)(pb + (i * 64 + lane) * 16);
#pragma unroll
      for (int i = 0; i < 3; ++i) {
        const int e = i * 64 + lane;
        if (e < 160) __builtin_amdgcn_raw_buffer_store_b128(v[i], rs, gb + (e >= 80 ? 20480 - 1280 : 0) + e * 16, 0, 16);
      }
      WAIT_L0();
      lds_signal(fb + F_PA, lane);
    }
    STAMP2(3);
    WAIT_V(0);
    STAMP2(4);
    if (lane == 0) {
      unsigned old;
      asm volatile("ds_add_rtn_u32 %0, %1, %2\n\ts_waitcnt lgkmcnt(0)" : "=v"(old) : "v"(fb + F_PUB), "v"(1u) : "memory");
      if (old == 3u) __hip_atomic_fetch_add(cnt + tg * 64, 1u, __ATOMIC_RELAXED, __HIP_MEMORY_SCOPE_AGENT);
    }
    __builtin_amdgcn_s_setprio(0);
    {
      const __amdgpu_buffer_rsrc_t rsa = __builtin_amdgcn_make_buffer_rsrc(ws + WS_OA, 0, 2621440, 0x00020000);
      const unsigned fo = (unsigned)((rt2 * 4 + j) * 20480 + lane * 16);
      f32x4 acc[5];
#pragma unroll
      for (int m = 0; m < 5; ++m) acc[m] = (f32x4){0.f, 0.f, 0.f, 0.f};
      s16x8 A[4][5];
      lds_wait(fb + F_FULL + 24, 2);
      lds_wait(fb + F_FULL + 28, 2);
#pragma unroll
      for (int n = 0; n < 4; ++n) k2_loadA<6, 4>(wa0, A, n);
      SBAR();
      if (j == 0) {
        GAS unsigned* cp = cnt + (rt2 >> 1) * 64;
        unsigned spins = 0;
        while ((unsigned)__builtin_amdgcn_readfirstlane(__hip_atomic_load(cp, __ATOMIC_RELAXED, __HIP_MEMORY_SCOPE_AGENT)) < 16u) {
          __builtin_amdgcn_s_sleep(2);
          if (++spins > (1u << 22)) break;
        }
        asm volatile("" ::: "memory");
        lds_signal(fb + F_GO, lane);
      } else {
        lds_wait(fb + F_GO, 1);
      }
      STAMP2(5);
      u32x4 Bf[10];
#pragma unroll
      for (int ks = 0; ks < 10; ++ks) Bf[ks] = __builtin_amdgcn_raw_buffer_load_b128(rsa, fo + (10 + ks) * 1024, 0, 16);
      SBAR();
      k2_gemm_a<6>(wa0, A, Bf, acc);
      lds_wait(fb + F_FREE + 8, 4);
#pragma unroll
      for (int m = 0; m < 5; ++m) *(LAS f32x4*)(sbase + K2_RED + j * 5120 + m * 1024 + lane * 16) = acc[m];
      WAIT_L0();
      lds_signal(fb + F_RED + 4 * j, lane);
    }
    STAMP2(7);
    return;
  }

  const unsigned fo = (unsigned)((rt2 * 4 + w) * 20480 + lane * 16);
  u32x4 Bf[20];
  {
    const __amdgpu_buffer_rsrc_t rk = __builtin_amdgcn_make_buffer_rsrc(ws + WS_KVP, 0, 860160, 0x00020000);
    const int t = tid < 210 ? tid : 209;
    u32x4 v[16];
#pragma unroll
    for (int r = 0; r < 16; ++r) v[r] = __builtin_amdgcn_raw_buffer_load_b128(rk, t * 16, hp * 3360 + r * 53760, 0);
    SBAR();
    BAR();
    SBAR();
#pragma unroll
    for (int ks = 0; ks < NB_EARLY; ++ks) Bf[ks] = *(const u32x4*)(ws + WS_OB + fo + ks * 1024);
    SBAR();
    float a[8];
#pragma unroll
    for (int jj = 0; jj < 8; ++jj) a[jj] = 0.f;
#pragma unroll
    for (int r = 0; r < 16; ++r)
#pragma unroll
      for (int jj = 0; jj < 4; ++jj) { a[2 * jj] += bflo(v[r][jj]); a[2 * jj + 1] += bfhi(v[r][jj]); }
    const int row = (t * 52429) >> 18, f8 = t - row * 5;
    ldsw128(sbase + K2_KV + row * 96 + f8 * 16, (u32x4){pack2(a[0], a[1]), pack2(a[2], a[3]), pack2(a[4], a[5]), pack2(a[6], a[7])});
    WAIT_L0();
    lds_signal(fb + F_KV, lane);
    SBAR();
#pragma unroll
    for (int ks = NB_EARLY; ks < 20; ++ks) Bf[ks] = *(const u32x4*)(ws + WS_OB + fo + ks * 1024);
    SBAR();
  }
  STAMP2(1);
  const int tok = rt2 * 64 + 16 * w + c;
  const float gt = *(const float*)(ws + WS_GATE + tok * 4);
  f32x4 acc[5];
#pragma unroll
  for (int m = 0; m < 5; ++m) acc[m] = (f32x4){0.f, 0.f, 0.f, 0.f};
  k2_gemm_b(wa0, fb, lane, Bf, acc);
#pragma unroll
  for (int m = 0; m < 5; ++m) acc[m] *= (1.f - gt);
  STAMP2(3);
  {
    const __amdgpu_buffer_rsrc_t rsa = __builtin_amdgcn_make_buffer_rsrc(ws + WS_OA, 0, 2621440, 0x00020000);
    s16x8 A[4][5];
    lds_wait(fb + F_FULL + 16, 2);
    lds_wait(fb + F_FULL + 20, 2);
#pragma unroll
    for (int n = 0; n < 4; ++n) k2_loadA<4, 4>(wa0, A, n);
    SBAR();
    lds_wait(fb + F_GO, 1);
    STAMP2(4);
    u32x4 Ba[10];
#pragma unroll
    for (int ks = 0; ks < 10; ++ks) Ba[ks] = __builtin_amdgcn_raw_buffer_load_b128(rsa, fo + ks * 1024, 0, 16);
    SBAR();
    k2_gemm_a<4>(wa0, A, Ba, acc);
    STAMP2(5);
    lds_wait(fb + F_RED + 4 * w, 1);
#pragma unroll
    for (int m = 0; m < 5; ++m) acc[m] += *(LAS f32x4*)(sbase + K2_RED + w * 5120 + m * 1024 + lane * 16);
  }
  STAMP2(6);
  {
    char* op = out + ((size_t)tok * 640 + ct * 80 + 4 * g) * 2;
#pragma unroll
    for (int m = 0; m < 5; ++m) *(u32x2*)(op + m * 32) = (u32x2){pack2(acc[m][0], acc[m][1]), pack2(acc[m][2], acc[m][3])};
  }
  STAMP2(7);
}

extern "C" void kernel_launch(void* const* d_in, const int* in_sizes, int n_in, void* d_out, int out_size, void* d_ws,
                              size_t ws_size, hipStream_t stream) {
  const char* x = (const char*)d_in[0];
  const char* wA = (const char*)d_in[1];
  const char* wphi = (const char*)d_in[2];
  const char* woA = (const char*)d_in[3];
  const char* wB = (const char*)d_in[5];
  const char* woB = (const char*)d_in[6];
  const char* gw = (const char*)d_in[7];
  (void)hipFuncSetAttribute((const void*)k1_kernel, hipFuncAttributeMaxDynamicSharedMemorySize, K1_LDS);
  (void)hipFuncSetAttribute((const void*)k2_kernel, hipFuncAttributeMaxDynamicSharedMemorySize, K2_LDS);
  k1_kernel<<<256, 512, K1_LDS, stream>>>(x, wA, wB, wphi, gw, woA, woB, (char*)d_ws);
  k2_kernel<<<256, 640, K2_LDS, stream>>>((char*)d_ws, (char*)d_out);
}
```

```cpp
#include <hip/hip_runtime.h>
#include <hip/hip_bf16.h>
#include <stdint.h>
#define CODE_PF 1
#define RAMP 0
#define SC1_MODE 1

typedef __attribute__((ext_vector_type(4))) short s16x4;
typedef __attribute__((ext_vector_type(8))) short s16x8;
typedef __attribute__((ext_vector_type(8))) __bf16 bf16x8;
typedef __attribute__((ext_vector_type(2))) __bf16 bf16x2;
typedef __attribute__((ext_vector_type(2))) float f32x2;
typedef __attribute__((ext_vector_type(4))) float f32x4;
typedef __attribute__((ext_vector_type(2))) unsigned u32x2;
typedef __attribute__((ext_vector_type(4))) unsigned u32x4;
#define LAS __attribute__((address_space(3)))
#define GAS __attribute__((address_space(1)))
#define DI __device__ __forceinline__

__device__ __attribute__((noinline, used)) void code_mark_a() { asm volatile("s_nop 0"); }
__device__ __attribute__((noinline, used)) void code_mark_b();

constexpr size_t WS_CNT = 0;
constexpr size_t WS_GATE = 4096;
constexpr size_t WS_PQ = 16384;
constexpr size_t WS_OB = WS_PQ + 2621440;
constexpr size_t WS_OA = WS_OB + 2621440;
constexpr size_t WS_KVP = WS_OA + 2621440;
constexpr size_t WS_WO = WS_KVP + 860160;

constexpr float LOG2E = 1.4426950408889634f;

DI unsigned pack2(float a, float b) {
  f32x2 v = {a, b};
  bf16x2 r = __builtin_convertvector(v, bf16x2);
  return __builtin_bit_cast(unsigned, r);
}
DI float rb(float a) { return (float)(__bf16)a; }
DI float bflo(unsigned u) { return __uint_as_float(u << 16); }
DI float bfhi(unsigned u) { return __uint_as_float(u & 0xffff0000u); }
DI float ex2(float a) { return __builtin_amdgcn_exp2f(a); }
DI float rcp(float a) { return __builtin_amdgcn_rcpf(a); }
DI float dot2bf(unsigned a, unsigned b, float c) {
  return __builtin_amdgcn_fdot2_f32_bf16(__builtin_bit_cast(bf16x2, a), __builtin_bit_cast(bf16x2, b), c, false);
}

DI s16x4 trr(unsigned addr) { return __builtin_amdgcn_ds_read_tr16_b64_v4i16((LAS s16x4*)addr); }
DI s16x8 cat8(s16x4 a, s16x4 b) { return __builtin_shufflevector(a, b, 0, 1, 2, 3, 4, 5, 6, 7); }
DI s16x8 ldsr128(unsigned addr) { return *(LAS s16x8*)addr; }
DI s16x4 ldsr64(unsigned addr) { return *(LAS s16x4*)addr; }
DI void ldsw128(unsigned addr, u32x4 v) { *(LAS u32x4*)addr = v; }
DI void ldsw64(unsigned addr, u32x2 v) { *(LAS u32x2*)addr = v; }
DI f32x4 mfma32(s16x8 a, s16x8 b, f32x4 c) {
  return __builtin_amdgcn_mfma_f32_16x16x32_bf16(__builtin_bit_cast(bf16x8, a), __builtin_bit_cast(bf16x8, b), c, 0, 0, 0);
}
DI f32x4 mfma16(s16x4 a, s16x4 b, f32x4 c) {
  const s16x4 z = {0, 0, 0, 0};
  return mfma32(cat8(a, z), cat8(b, z), c);
}
DI s16x8 mk8(u32x2 a, u32x2 b) {
  u32x4 v = {a[0], a[1], b[0], b[1]};
  return __builtin_bit_cast(s16x8, v);
}
DI void glds16(const char* base, unsigned off, unsigned lds) {
  __builtin_amdgcn_global_load_lds(base + off, (LAS void*)lds, 16, 0, 0);
}
DI float xmax4(float v) {
  auto a = __builtin_amdgcn_permlane16_swap(__float_as_uint(v), __float_as_uint(v), false, false);
  v = fmaxf(__uint_as_float(a[0]), __uint_as_float(a[1]));
  auto b = __builtin_amdgcn_permlane32_swap(__float_as_uint(v), __float_as_uint(v), false, false);
  return fmaxf(__uint_as_float(b[0]), __uint_as_float(b[1]));
}
DI float xsum4(float v) {
  auto a = __builtin_amdgcn_permlane16_swap(__float_as_uint(v), __float_as_uint(v), false, false);
  v = __uint_as_float(a[0]) + __uint_as_float(a[1]);
  auto b = __builtin_amdgcn_permlane32_swap(__float_as_uint(v), __float_as_uint(v), false, false);
  return __uint_as_float(b[0]) + __uint_as_float(b[1]);
}
#define WAIT_V(n) asm volatile("s_waitcnt vmcnt(" #n ")" ::: "memory")
#define WAIT_L0() do { asm volatile("" ::: "memory"); __builtin_amdgcn_s_waitcnt(0xc07f); asm volatile("" ::: "memory"); } while (0)
#define BAR() do { asm volatile("" ::: "memory"); __builtin_amdgcn_s_barrier(); asm volatile("" ::: "memory"); } while (0)
#define SBAR() __builtin_amdgcn_sched_barrier(0)
#define SGB(mask, n) __builtin_amdgcn_sched_group_barrier(mask, n, 0)

#define STAMP(i)
#define STAMP2(i)

#define ST_AUX 16
DI __amdgpu_buffer_rsrc_t ws_rsrc(char* ws) { return __builtin_amdgcn_make_buffer_rsrc(ws, 0, 16 << 20, 0x00020000); }
DI void st128(__amdgpu_buffer_rsrc_t r, unsigned off, u32x4 v) { __builtin_amdgcn_raw_buffer_store_b128(v, r, off, 0, ST_AUX); }
DI void st64(__amdgpu_buffer_rsrc_t r, unsigned off, u32x2 v) { __builtin_amdgcn_raw_buffer_store_b64(v, r, off, 0, ST_AUX); }

DI void dual_softmax(const float (&p)[20], unsigned (&ph)[20]) {
  float mx = p[0], mn = p[0];
#pragma unroll
  for (int i = 1; i < 20; ++i) { mx = fmaxf(mx, p[i]); mn = fminf(mn, p[i]); }
  const f32x2 bp = {-mx * LOG2E, -mx * LOG2E}, bn = {mn * LOG2E, mn * LOG2E};
  const f32x2 cp = {LOG2E, LOG2E}, cn = {-LOG2E, -LOG2E};
  f32x2 ep[10], en[10], sp = {0.f, 0.f}, sn = {0.f, 0.f};
#pragma unroll
  for (int i = 0; i < 10; ++i) {
    const f32x2 v = {p[2 * i], p[2 * i + 1]};
    const f32x2 a = __builtin_elementwise_fma(v, cp, bp), c = __builtin_elementwise_fma(v, cn, bn);
    ep[i] = (f32x2){ex2(a[0]), ex2(a[1])};
    en[i] = (f32x2){ex2(c[0]), ex2(c[1])};
    sp += ep[i];
    sn += en[i];
  }
  const float rp = rcp(sp[0] + sp[1]), rn = rcp(sn[0] + sn[1]);
#pragma unroll
  for (int i = 0; i < 10; ++i) {
    const f32x2 a = ep[i] * rp, c = en[i] * rn;
    ph[i] = pack2(a[0], a[1]);
    ph[10 + i] = pack2(c[0], c[1]);
  }
}

#define LD_DELAY 8
#define NB_EARLY 8
constexpr int K2_WST = 25600;
constexpr int K2_PHI = 5 * K2_WST;
constexpr int K2_RED = 2 * K2_WST;
constexpr int K2_KV = 6 * K2_WST;
constexpr int K2_FLG = K2_KV + 4096;
constexpr int K2_LDS = K2_FLG + 128;
constexpr int F_FULL = 0, F_FREE = 32, F_KV = 64, F_PA = 68, F_GO = 72, F_PUB = 76, F_RED = 80;

DI void lds_signal(unsigned addr, int lane) {
  if (lane == 0) asm volatile("ds_add_u32 %0, %1" :: "v"(addr), "v"(1u) : "memory");
}
DI void lds_wait(unsigned addr, unsigned want) {
#pragma nounroll
  for (unsigned spins = 0; spins < (1u << 24); ++spins) {
    unsigned v;
    asm volatile("ds_read_b32 %0, %1\n\ts_waitcnt lgkmcnt(0)" : "=v"(v) : "v"(addr) : "memory");
    if ((unsigned)__builtin_amdgcn_readfirstlane(v) >= want) break;
    __builtin_amdgcn_s_sleep(1);
  }
}

template <int S0, int NB>
DI void k2_loadA(unsigned wa0, s16x8 (&A)[NB][5], int n) {
  const int s = S0 + n / 5, ks = n % 5;
#pragma unroll
  for (int m = 0; m < 5; ++m) {
    A[n % NB][m] = ldsr128(wa0 + (s % 6) * K2_WST + ks * 5120 + m * 1024);
  }
}
DI void k2_gemm_b(unsigned wa0, unsigned fb, int lane, const u32x4 (&Bf)[20], f32x4 (&acc)[5]) {
  constexpr int NB = 3;
  s16x8 A[NB][5];
  lds_wait(fb + F_FULL + 0, 2);
  k2_loadA<0, NB>(wa0, A, 0);
  k2_loadA<0, NB>(wa0, A, 1);
#pragma unroll
  for (int n = 0; n < 20; ++n) {
    if (n + 2 < 20) {
      if ((n + 2) % 5 == 0) {
        lds_wait(fb + F_FULL + 4 * ((n + 2) / 5), 2);
        lds_signal(fb + F_FREE + 4 * ((n + 2) / 5 - 1), lane);
      }
      k2_loadA<0, NB>(wa0, A, n + 2);
    }
    SBAR();
#pragma unroll
    for (int m = 0; m < 5; ++m) acc[m] = mfma32(A[n % NB][m], __builtin_bit_cast(s16x8, Bf[n]), acc[m]);
    SBAR();
  }
}
template <int S0>
DI void k2_gemm_a(unsigned wa0, s16x8 (&A)[4][5], const u32x4 (&Bf)[10], f32x4 (&acc)[5]) {
#pragma unroll
  for (int n = 0; n < 10; ++n) {
#pragma unroll
    for (int m = 0; m < 5; ++m) acc[m] = mfma32(A[n % 4][m], __builtin_bit_cast(s16x8, Bf[n]), acc[m]);
    SBAR();
    if (n + 4 < 10) k2_loadA<S0, 4>(wa0, A, n + 4);
    SBAR();
  }
}

__global__ void __launch_bounds__(640, 3)
k2_kernel(char* __restrict__ ws, char* __restrict__ out) {
  extern __shared__ __attribute__((aligned(16))) char smem[];
  const unsigned sbase = (unsigned)(size_t)(LAS char*)smem;
  const int tid = threadIdx.x, lane = tid & 63;
  const int w = __builtin_amdgcn_readfirstlane(tid >> 6);
  const int b = blockIdx.x, xcd = b & 7, idx = b >> 3;
  const int rt2 = (xcd >> 1) * 8 + (idx & 7);
  const int ct = (xcd & 1) * 4 + (idx >> 3);
  const int g = lane >> 4, c = lane & 15, qq = (lane >> 2) & 3, pp = lane & 3;
  const int hp = xcd * 2 + (idx >> 4), tg = idx & 15;
  GAS unsigned* cnt = (GAS unsigned*)(ws + WS_CNT);
  const unsigned fb = sbase + K2_FLG;
  const unsigned wa0 = sbase + lane * 16;
  STAMP2(0);
  if (w == 8 && lane < 32) asm volatile("ds_write_b32 %0, %1\n\ts_waitcnt lgkmcnt(0)" :: "v"(fb + lane * 4), "v"(0u) : "memory");
  BAR();

  if (w >= 8) {
    const int lw = w - 8;
    const char* Wp = ws + WS_WO + (size_t)ct * 204800;
    const unsigned woff = (unsigned)(lw * 1024 + lane * 16);
    auto issueW = [&](int s) {
      const unsigned lb = sbase + (s % 6) * K2_WST + lw * 1024;
#pragma unroll
      for (int i = 0; i < 12; ++i) glds16(Wp + s * K2_WST, woff + i * 2048, lb + i * 2048);
      if (lw == 0) glds16(Wp + s * K2_WST, woff + 24576, lb + 24576);
    };
    __builtin_amdgcn_s_sleep(LD_DELAY);
    STAMP2(1);
    issueW(0); issueW(1); issueW(2);
    WAIT_V(24); lds_signal(fb + F_FULL + 0, lane);
    STAMP2(2);
    issueW(3);
    WAIT_V(24); lds_signal(fb + F_FULL + 4, lane);
    issueW(4);
    WAIT_V(24); lds_signal(fb + F_FULL + 8, lane);
    WAIT_V(12); lds_signal(fb + F_FULL + 12, lane);
    WAIT_V(0); lds_signal(fb + F_FULL + 16, lane);
    lds_wait(fb + F_FREE + 0, 4);
    issueW(6);
    lds_wait(fb + F_PA, 4);
    issueW(5);
    lds_wait(fb + F_FREE + 4, 4);
    issueW(7);
    WAIT_V(24); lds_signal(fb + F_FULL + 24, lane);
    WAIT_V(12); lds_signal(fb + F_FULL + 20, lane);
    WAIT_V(0); lds_signal(fb + F_FULL + 28, lane);
    STAMP2(4);
    STAMP2(7);
    return;
  }

  if (w >= 4) {
    __builtin_amdgcn_s_setprio(3);
    const int j = w - 4;
    const u32x2* pqp = (const u32x2*)(ws + WS_PQ + ((size_t)(hp * 2 + (lane >> 5)) * 2048 + tg * 128 + 32 * j + (lane & 31)) * 40);
    u32x2 pv[5];
#pragma unroll
    for (int i = 0; i < 5; ++i) pv[i] = pqp[i];
    float gta[2];
#pragma unroll
    for (int tt = 0; tt < 2; ++tt) gta[tt] = *(const float*)(ws + WS_GATE + (tg * 128 + 32 * j + 16 * tt + c) * 4);
    SBAR();
    const unsigned pb = sbase + K2_PHI + j * 5120;
    {
      float pk[20];
#pragma unroll
      for (int i = 0; i < 5; ++i) {
        pk[4 * i] = bflo(pv[i][0]); pk[4 * i + 1] = bfhi(pv[i][0]);
        pk[4 * i + 2] = bflo(pv[i][1]); pk[4 * i + 3] = bfhi(pv[i][1]);
      }
      unsigned ph[20];
      dual_softmax(pk, ph);
#pragma unroll
      for (int i = 0; i < 5; ++i) ldsw128(pb + lane * 80 + 16 * i, (u32x4){ph[4 * i], ph[4 * i + 1], ph[4 * i + 2], ph[4 * i + 3]});
    }
    WAIT_L0();
    s16x8 PB0[2][2];
    s16x4 PB1[2][2];
#pragma unroll
    for (int hh = 0; hh < 2; ++hh)
#pragma unroll
      for (int tt = 0; tt < 2; ++tt) {
        const unsigned pa = pb + (hh * 32 + tt * 16 + c) * 80;
        PB0[hh][tt] = ldsr128(pa + g * 16);
        s16x4 t1 = ldsr64(pa + 64 + (g & 1) * 8);
        const s16x4 z = {0, 0, 0, 0};
        PB1[hh][tt] = g < 2 ? t1 : z;
      }
    STAMP2(1);
    lds_wait(fb + F_KV, 4);
    STAMP2(2);
    const int r1 = c < 4 ? 16 + c : 20;
    const f32x4 z4 = (f32x4){0.f, 0.f, 0.f, 0.f};
    s16x8 KA0[2][2];
    s16x4 KA1[2][2];
#pragma unroll
    for (int hh = 0; hh < 2; ++hh) {
      const unsigned kb = sbase + K2_KV + hh * 21 * 96;
      KA0[hh][0] = ldsr128(kb + c * 96 + g * 16);
      KA0[hh][1] = ldsr128(kb + r1 * 96 + g * 16);
      KA1[hh][0] = ldsr64(kb + c * 96 + 64 + (g & 1) * 8);
      KA1[hh][1] = ldsr64(kb + r1 * 96 + 64 + (g & 1) * 8);
    }
    f32x4 n0[2][2], n1[2][2];
#pragma unroll
    for (int hh = 0; hh < 2; ++hh)
#pragma unroll
      for (int tt = 0; tt < 2; ++tt) {
        n1[hh][tt] = mfma32(KA0[hh][1], PB0[hh][tt], z4);
        n0[hh][tt] = mfma32(KA0[hh][0], PB0[hh][tt], z4);
      }
#pragma unroll
    for (int hh = 0; hh < 2; ++hh)
#pragma unroll
      for (int tt = 0; tt < 2; ++tt) {
        n1[hh][tt] = mfma16(KA1[hh][1], PB1[hh][tt], n1[hh][tt]);
        n0[hh][tt] = mfma16(KA1[hh][0], PB1[hh][tt], n0[hh][tt]);
      }
#pragma unroll
    for (int hh = 0; hh < 2; ++hh)
#pragma unroll
      for (int tt = 0; tt < 2; ++tt) {
        auto sw = __builtin_amdgcn_permlane16_swap(__float_as_uint(n1[hh][tt][0]), __float_as_uint(n1[hh][tt][0]), false, false);
        const float den = __uint_as_float(sw[1]);
        const float rd = gta[tt] * rcp(den + 1e-6f);
        const int e0 = hh * 20 + 4 * g, e1 = hh * 20 + 16;
        const unsigned oa = pb + tt * 1280 + c * 16;
        const f32x4 a = n0[hh][tt] * rd, bq = n1[hh][tt] * rd;
        ldsw64(oa + (e0 >> 3) * 256 + (e0 & 7) * 2, (u32x2){pack2(a[0], a[1]), pack2(a[2], a[3])});
        ldsw64(g == 0 ? oa + (e1 >> 3) * 256 + (e1 & 7) * 2 : pb + 4096 + lane * 8, (u32x2){pack2(bq[0], bq[1]), pack2(bq[2], bq[3])});
      }
    WAIT_L0();
    STAMP2(8);
    {
      const __amdgpu_buffer_rsrc_t rs = __builtin_amdgcn_make_buffer_rsrc(ws + WS_OA, 0, 2621440, 0x00020000);
      const unsigned gb = (unsigned)((tg * 8 + j * 2) * 20480 + hp * 1280);
      u32x4 v[3];
#pragma unroll
      for (int i = 0; i < 3; ++i) v[i] = *(LAS u32x4*)(pb + (i * 64 + lane) * 16);
#pragma unroll
      for (int i = 0; i < 3; ++i) {
        const int e = i * 64 + lane;
        if (e < 160) __builtin_amdgcn_raw_buffer_store_b128(v[i], rs, gb + (e >= 80 ? 20480 - 1280 : 0) + e * 16, 0, 16);
      }
      WAIT_L0();
      lds_signal(fb + F_PA, lane);
    }
    STAMP2(3);
    WAIT_V(0);
    STAMP2(4);
    if (lane == 0) {
      unsigned old;
      asm volatile("ds_add_rtn_u32 %0, %1, %2\n\ts_waitcnt lgkmcnt(0)" : "=v"(old) : "v"(fb + F_PUB), "v"(1u) : "memory");
      if (old == 3u) __hip_atomic_fetch_add(cnt + tg * 64, 1u, __ATOMIC_RELAXED, __HIP_MEMORY_SCOPE_AGENT);
    }
    __builtin_amdgcn_s_setprio(0);
    {
      const __amdgpu_buffer_rsrc_t rsa = __builtin_amdgcn_make_buffer_rsrc(ws + WS_OA, 0, 2621440, 0x00020000);
      const unsigned fo = (unsigned)((rt2 * 4 + j) * 20480 + lane * 16);
      f32x4 acc[5];
#pragma unroll
      for (int m = 0; m < 5; ++m) acc[m] = (f32x4){0.f, 0.f, 0.f, 0.f};
      s16x8 A[4][5];
      lds_wait(fb + F_FULL + 24, 2);
      lds_wait(fb + F_FULL + 28, 2);
#pragma unroll
      for (int n = 0; n < 4; ++n) k2_loadA<6, 4>(wa0, A, n);
      SBAR();
      if (j == 0) {
        GAS unsigned* cp = cnt + (rt2 >> 1) * 64;
        unsigned spins = 0;
        while ((unsigned)__builtin_amdgcn_readfirstlane(__hip_atomic_load(cp, __ATOMIC_RELAXED, __HIP_MEMORY_SCOPE_AGENT)) < 16u) {
          __builtin_amdgcn_s_sleep(2);
          if (++spins > (1u << 22)) break;
        }
        asm volatile("" ::: "memory");
        lds_signal(fb + F_GO, lane);
      } else {
        lds_wait(fb + F_GO, 1);
      }
      STAMP2(5);
      u32x4 Bf[10];
#pragma unroll
      for (int ks = 0; ks < 10; ++ks) Bf[ks] = __builtin_amdgcn_raw_buffer_load_b128(rsa, fo + (10 + ks) * 1024, 0, 16);
      SBAR();
      k2_gemm_a<6>(wa0, A, Bf, acc);
      lds_wait(fb + F_FREE + 8, 4);
#pragma unroll
      for (int m = 0; m < 5; ++m) *(LAS f32x4*)(sbase + K2_RED + j * 5120 + m * 1024 + lane * 16) = acc[m];
      WAIT_L0();
      lds_signal(fb + F_RED + 4 * j, lane);
    }
    STAMP2(7);
    return;
  }

  const unsigned fo = (unsigned)((rt2 * 4 + w) * 20480 + lane * 16);
  u32x4 Bf[20];
  {
    const __amdgpu_buffer_rsrc_t rk = __builtin_amdgcn_make_buffer_rsrc(ws + WS_KVP, 0, 860160, 0x00020000);
    const int t = tid < 210 ? tid : 209;
    u32x4 v[16];
#pragma unroll
    for (int r = 0; r < 16; ++r) v[r] = __builtin_amdgcn_raw_buffer_load_b128(rk, t * 16, hp * 3360 + r * 53760, 0);
    SBAR();
#pragma unroll
    for (int ks = 0; ks < NB_EARLY; ++ks) Bf[ks] = *(const u32x4*)(ws + WS_OB + fo + ks * 1024);
    SBAR();
    float a[8];
#pragma unroll
    for (int jj = 0; jj < 8; ++jj) a[jj] = 0.f;
#pragma unroll
    for (int r = 0; r < 16; ++r)
#pragma unroll
      for (int jj = 0; jj < 4; ++jj) { a[2 * jj] += bflo(v[r][jj]); a[2 * jj + 1] += bfhi(v[r][jj]); }
    const int row = (t * 52429) >> 18, f8 = t - row * 5;
    ldsw128(sbase + K2_KV + row * 96 + f8 * 16, (u32x4){pack2(a[0], a[1]), pack2(a[2], a[3]), pack2(a[4], a[5]), pack2(a[6], a[7])});
    WAIT_L0();
    lds_signal(fb + F_KV, lane);
    SBAR();
#pragma unroll
    for (int ks = NB_EARLY; ks < 20; ++ks) Bf[ks] = *(const u32x4*)(ws + WS_OB + fo + ks * 1024);
    SBAR();
  }
  STAMP2(1);
  const int tok = rt2 * 64 + 16 * w + c;
  const float gt = *(const float*)(ws + WS_GATE + tok * 4);
  f32x4 acc[5];
#pragma unroll
  for (int m = 0; m < 5; ++m) acc[m] = (f32x4){0.f, 0.f, 0.f, 0.f};
  k2_gemm_b(wa0, fb, lane, Bf, acc);
#pragma unroll
  for (int m = 0; m < 5; ++m) acc[m] *= (1.f - gt);
  STAMP2(3);
  {
    const __amdgpu_buffer_rsrc_t rsa = __builtin_amdgcn_make_buffer_rsrc(ws + WS_OA, 0, 2621440, 0x00020000);
    s16x8 A[4][5];
    lds_wait(fb + F_FULL + 16, 2);
    lds_wait(fb + F_FULL + 20, 2);
#pragma unroll
    for (int n = 0; n < 4; ++n) k2_loadA<4, 4>(wa0, A, n);
    SBAR();
    lds_wait(fb + F_GO, 1);
    STAMP2(4);
    u32x4 Ba[10];
#pragma unroll
    for (int ks = 0; ks < 10; ++ks) Ba[ks] = __builtin_amdgcn_raw_buffer_load_b128(rsa, fo + ks * 1024, 0, 16);
    SBAR();
    k2_gemm_a<4>(wa0, A, Ba, acc);
    STAMP2(5);
    lds_wait(fb + F_RED + 4 * w, 1);
#pragma unroll
    for (int m = 0; m < 5; ++m) acc[m] += *(LAS f32x4*)(sbase + K2_RED + w * 5120 + m * 1024 + lane * 16);
  }
  STAMP2(6);
  {
    char* op = out + ((size_t)tok * 640 + ct * 80 + 4 * g) * 2;
#pragma unroll
    for (int m = 0; m < 5; ++m) *(u32x2*)(op + m * 32) = (u32x2){pack2(acc[m][0], acc[m][1]), pack2(acc[m][2], acc[m][3])};
  }
  STAMP2(7);
}

constexpr int K1_WH = 15360;
constexpr int K1_XB = 6 * K1_WH;
constexpr int K1_XST = 18432;
constexpr int K1_WPHI = K1_XB + 3 * K1_XST;
constexpr int K1_ZERO = K1_WPHI + 4096;
constexpr int K1_RP = K1_ZERO + 1024;
constexpr int K1_LDS = K1_RP + 10240;
constexpr int K1_OST = 98304;
constexpr int K1_QK = 81920;

constexpr int ftoff(int ft) { return (ft / 5) * 160 + (ft % 5) * 8; }

template <int FH, int HM>
DI void k1_consumer_main(unsigned sbase, int th, int lane, f32x4 (&acc)[8][4], f32x4 (&acch)[8], char* ws) {
  constexpr int NT = FH ? 7 : 8;
  const int g = lane >> 4, c = lane & 15, qq = (lane >> 2) & 3, pp = lane & 3;
  const unsigned wa0 = sbase + (16 * (g >> 1) + 4 * (g & 1) + qq) * 480 + pp * 40;
  unsigned xh0[2], xb0[2];
#pragma unroll
  for (int a = 0; a < 2; ++a) {
    xh0[a] = sbase + K1_XB + c * 128 + (((4 * a + g) ^ (c & 7)) * 16);
    xb0[a] = xh0[a] + (16 + 64 * th) * 128;
  }
#pragma unroll
  for (int m = 0; m < 8; ++m) {
#pragma unroll
    for (int t = 0; t < 4; ++t) acc[m][t] = (f32x4){0.f, 0.f, 0.f, 0.f};
    acch[m] = (f32x4){0.f, 0.f, 0.f, 0.f};
  }
  s16x8 A[2][4], B[2][4], BH[2];
  auto loadA = [&](int grp, unsigned woff) {
#pragma unroll
    for (int m = 0; m < 4; ++m)
      if (4 * grp + m < NT) {
        const unsigned ad = wa0 + woff + ftoff(8 * FH + 4 * grp + m);
        A[grp][m] = cat8(trr(ad), trr(ad + 3840));
      }
  };
  auto loadB = [&](int buf, unsigned xoff) {
#pragma unroll
    for (int t = 0; t < 4; ++t) B[buf][t] = ldsr128(xb0[buf] + xoff + t * 2048);
    if (HM) BH[buf] = ldsr128(xh0[buf] + xoff);
  };
  auto mm = [&](int grp, int buf) {
#pragma unroll
    for (int m = 0; m < 4; ++m)
      if (4 * grp + m < NT) {
#pragma unroll
        for (int t = 0; t < 4; ++t) acc[4 * grp + m][t] = mfma32(A[grp][m], B[buf][t], acc[4 * grp + m][t]);
        if ((HM >> (4 * grp + m)) & 1) acch[4 * grp + m] = mfma32(A[grp][m], BH[buf], acch[4 * grp + m]);
      }
  };
  auto kstep = [&](int a, unsigned woff, unsigned nwoff, unsigned nxoff, bool last) {
    loadA(1, woff);
    mm(0, a);
    SGB(0x8, 1); SGB(0x100, 2); SGB(0x8, 1); SGB(0x100, 2); SGB(0x8, 1); SGB(0x100, 2); SGB(0x8, 1); SGB(0x100, 2);
    SBAR();
    WAIT_L0();
    BAR();
    if (!last) {
      loadB(a ^ 1, nxoff);
      loadA(0, nwoff);
    }
    mm(1, a);
    if (!last) {
      SGB(0x8, 1); SGB(0x100, 2); SGB(0x8, 1); SGB(0x100, 2); SGB(0x8, 1); SGB(0x100, 2); SGB(0x8, 1); SGB(0x100, 2);
      SGB(0x8, 1); SGB(0x100, 2); SGB(0x8, 1); SGB(0x100, 2); SGB(0x8, 1); SGB(0x100, 1);
    }
    SBAR();
  };
  STAMP(1);
  BAR();
  STAMP(2);
  loadB(0, 0);
  loadA(0, 0);
  SBAR();
  unsigned woff = 0, xoff = 0;
#pragma unroll 1
  for (int s = 0; s < 9; ++s) {
    unsigned nw = woff + 2 * K1_WH, nx = xoff + K1_XST;
    if (nw >= 6 * K1_WH) { nw = 0; nx = 0; }
    kstep(0, woff, woff + K1_WH, xoff, false);
    kstep(1, woff + K1_WH, nw, nx, false);
    woff = nw; xoff = nx;
  }
  kstep(0, 0, K1_WH, 0, false);
  kstep(1, K1_WH, 0, 0, true);
}

template <int FH, int HM, bool ISB>
DI void k1_dump(unsigned sbase, int th, int lane, const f32x4 (&acc)[8][4], const f32x4 (&acch)[8]) {
  constexpr int NT = FH ? 7 : 8;
  const int g = lane >> 4, c = lane & 15;
#pragma unroll
  for (int t = 0; t < 4; ++t) {
    u32x2 v[8];
#pragma unroll
    for (int m = 0; m < NT; ++m) v[m] = (u32x2){pack2(acc[m][t][0], acc[m][t][1]), pack2(acc[m][t][2], acc[m][t][3])};
    const int tok = 64 * th + 16 * t + c;
    if (ISB) {
      const unsigned rowa = sbase + (g * 144 + 16 + tok) * 160;
      if (FH == 0) {
        ldsw128(rowa + 0, (u32x4){v[0][0], v[0][1], v[1][0], v[1][1]});
        ldsw128(rowa + 16, (u32x4){v[2][0], v[2][1], v[3][0], v[3][1]});
        ldsw128(rowa + 32, (u32x4){v[4][0], v[4][1], 0u, 0u});
        ldsw128(rowa + 48, (u32x4){0u, 0u, 0u, 0u});
        ldsw128(rowa + 64, (u32x4){v[5][0], v[5][1], v[6][0], v[6][1]});
        ldsw64(rowa + 80, v[7]);
      } else {
        ldsw64(rowa + 88, v[0]);
        ldsw128(rowa + 96, (u32x4){v[1][0], v[1][1], v[2][0], v[2][1]});
        ldsw128(rowa + 112, (u32x4){v[3][0], v[3][1], v[4][0], v[4][1]});
        ldsw128(rowa + 128, (u32x4){v[5][0], v[5][1], v[6][0], v[6][1]});
      }
    } else {
      const unsigned qa = sbase + K1_QK + (4 * th + t) * 5120 + lane * 16;
      if (FH == 0) {
#pragma unroll
        for (int i = 0; i < 4; ++i) ldsw128(qa + i * 1024, (u32x4){v[2 * i][0], v[2 * i][1], v[2 * i + 1][0], v[2 * i + 1][1]});
      } else {
        ldsw128(qa + 4096, (u32x4){v[0][0], v[0][1], v[1][0], v[1][1]});
        const unsigned rowa = sbase + (g * 128 + tok) * 160;
        ldsw128(rowa + 80, (u32x4){v[2][0], v[2][1], v[3][0], v[3][1]});
        ldsw128(rowa + 96, (u32x4){v[4][0], v[4][1], v[5][0], v[5][1]});
        ldsw128(rowa + 112, (u32x4){v[6][0], v[6][1], 0x00003f80u, 0u});
      }
    }
  }
  if (ISB) {
#pragma unroll
    for (int m = 0; m < NT; ++m)
      if ((HM >> m) & 1)
        ldsw64(sbase + (g * 144 + c) * 160 + 64 + (8 * FH + m - 5) * 8, (u32x2){pack2(acch[m][0], acch[m][1]), pack2(acch[m][2], acch[m][3])});
  }
}

template <int FH, int HM, bool ISB>
DI void k1_consumer(unsigned sbase, int th, int lane, char* ws) {
  f32x4 acc[8][4], acch[8];
  k1_consumer_main<FH, HM>(sbase, th, lane, acc, acch, ws);
  STAMP(3);
  k1_dump<FH, HM, ISB>(sbase, th, lane, acc, acch);
}

__global__ void __launch_bounds__(512, 2)
k1_kernel(const char* __restrict__ x, const char* __restrict__ wA, const char* __restrict__ wB,
          const char* __restrict__ wphi, const char* __restrict__ gatew, const char* __restrict__ woA,
          const char* __restrict__ woB, char* __restrict__ ws) {
  extern __shared__ __attribute__((aligned(16))) char smem[];
  const unsigned sbase = (unsigned)(size_t)(LAS char*)smem;
  const int tid = threadIdx.x, lane = tid & 63;
  const int w = __builtin_amdgcn_readfirstlane(tid >> 6);
  const int b = blockIdx.x, xcd = b & 7, idx = b >> 3;
  const int rt = (xcd & 1) * 8 + (idx & 7);
  const int cg = (xcd >> 1) * 4 + (idx >> 3);
  const bool isB = cg >= 8;
  const int hg = cg & 7;
  const int row0 = rt * 128;
  const int g = lane >> 4, c = lane & 15, qq = (lane >> 2) & 3, pp = lane & 3;
  STAMP(0);
  if (w == 7) {
    unsigned pf[5], pfc = 0;
    {
      const int l2 = 64 + lane;
      pf[0] = *(const unsigned*)(x + (size_t)(b * 80 + lane) * 128);
      pf[1] = *(const unsigned*)(wA + (size_t)(b * 75 + lane) * 128);
      pf[2] = *(const unsigned*)(wB + (size_t)(b * 75 + lane) * 128);
      const char* p3 = lane < 16 ? x + (size_t)(b * 80 + l2) * 128
                     : (lane < 27 ? wA + (size_t)(b * 75 + 48 + lane) * 128 : wB + (size_t)(b * 75 + 37 + (lane < 38 ? lane : 37)) * 128);
      pf[3] = *(const unsigned*)p3;
      const char* p4 = lane < 25 ? woA + (size_t)(b * 25 + lane) * 128 : woB + (size_t)(b * 25 + (lane < 50 ? lane - 25 : 24)) * 128;
      pf[4] = *(const unsigned*)p4;
    }
    {
      const uintptr_t pa = (uintptr_t)&code_mark_a, pb2 = (uintptr_t)&code_mark_b;
      const uintptr_t lo = pa < pb2 ? pa : pb2, hi = pa < pb2 ? pb2 : pa;
      uintptr_t ad = (lo & ~(uintptr_t)127) + (uintptr_t)(idx * 16 + (lane & 15)) * 128;
      ad = ad < hi ? ad : lo;
      pfc = *(const GAS unsigned*)(ad & ~(uintptr_t)3);
    }
    SBAR();
    BAR(); BAR(); BAR(); BAR();
#pragma unroll
    for (int i = 0; i < 4; ++i) {
      int e = i * 64 + lane;
      e = e > 199 ? 199 : e;
      glds16(wphi + hg * 3200, (unsigned)e * 16u, sbase + K1_WPHI + i * 1024);
    }
#pragma unroll
    for (int r = 0; r < 2; ++r) {
      const int u = r == 0 ? b : (b < 64 ? 256 + b : b);
      const int uct = u / 40, uks = u - uct * 40;
      const char* wsrc = (uks < 20 ? woB + (size_t)uks * 40960 : woA + (size_t)(uks - 20) * 40960) + uct * 160;
#pragma unroll
      for (int i = 0; i < 5; ++i) {
        const int e = i * 64 + lane;
        const int rho = (e * 6554) >> 16, ch = e - rho * 10;
        const int kap = (rho & ~12) | ((rho & 4) << 1) | ((rho & 8) >> 1);
        glds16(wsrc, (unsigned)(kap * 1280 + ch * 16), sbase + K1_RP + r * 5120 + i * 1024);
      }
    }
    asm volatile("ds_write_b128 %0, %1" :: "v"(sbase + K1_ZERO + lane * 16), "v"((u32x4){0u, 0u, 0u, 0u}) : "memory");
    if (b == 0 && lane < 16) *(unsigned*)(ws + WS_CNT + lane * 256) = 0u;
    const int gtok = row0 + cg * 8 + (lane >> 3);
    u32x4 xv[10], gv[10];
#pragma unroll
    for (int i = 0; i < 10; ++i) {
      gv[i] = *(const u32x4*)(gatew + (lane & 7) * 160 + i * 16);
      xv[i] = *(const u32x4*)(x + (size_t)gtok * 1280 + (lane & 7) * 160 + i * 16);
    }
    SBAR();
#pragma unroll 1
    for (int i = 0; i < 7; ++i) BAR();
    SBAR();
    asm volatile("" :: "v"(pf[0]), "v"(pf[1]), "v"(pf[2]), "v"(pf[3]), "v"(pf[4]), "v"(pfc));
    {
      float a0 = 0.f, a1 = 0.f;
#pragma unroll
      for (int i = 0; i < 10; ++i)
#pragma unroll
        for (int j = 0; j < 4; j += 2) {
          a0 = dot2bf(xv[i][j], gv[i][j], a0);
          a1 = dot2bf(xv[i][j + 1], gv[i][j + 1], a1);
        }
      float a = a0 + a1;
      BAR();
      a += __int_as_float(__builtin_amdgcn_update_dpp(0, __float_as_int(a), 0xB1, 0xf, 0xf, true));
      a += __int_as_float(__builtin_amdgcn_update_dpp(0, __float_as_int(a), 0x4E, 0xf, 0xf, true));
      a += __int_as_float(__builtin_amdgcn_update_dpp(0, __float_as_int(a), 0x141, 0xf, 0xf, true));
      if ((lane & 7) == 0) *(float*)(ws + WS_GATE + gtok * 4) = rcp(1.f + ex2(-a * LOG2E));
      BAR();
    }
#pragma unroll
    for (int r = 0; r < 2; ++r) {
      const int u = r == 0 ? b : 256 + b;
      const unsigned ra = sbase + K1_RP + r * 5120 + (16 * (g >> 1) + 4 * (g & 1) + qq) * 160 + pp * 8;
      s16x8 F[5];
#pragma unroll
      for (int m = 0; m < 5; ++m) F[m] = cat8(trr(ra + m * 32), trr(ra + m * 32 + 1280));
      BAR();
      if (r == 0 || b < 64) {
#pragma unroll
        for (int m = 0; m < 5; ++m) {
          st128(ws_rsrc(ws), (unsigned)(WS_WO + u * 5120 + m * 1024 + lane * 16), __builtin_bit_cast(u32x4, F[m]));
        }
      }
      BAR();
    }
    BAR(); BAR();
    WAIT_L0();
    BAR(); BAR();
  } else if (w >= 4) {
    const int lw = w - 4;
    const char* Wb = (isB ? wB : wA) + hg * 160;
    const char* Xb = x;
    unsigned xoff[6], woff[5];
    {
      const int l8 = lane >> 3, slot = lane & 7;
      const unsigned sw = (unsigned)((slot ^ l8) * 16);
#pragma unroll
      for (int i = 0; i < 6; ++i) {
        int tok = row0 - 16 + (3 * i + lw) * 8 + l8;
        tok = tok < 0 ? 0 : tok;
        xoff[i] = (unsigned)tok * 1280u + sw;
      }
    }
    {
      const int u0 = lw * 64 + lane;
      int rho = (u0 * 2185) >> 16;
      int ch = u0 - rho * 30;
#pragma unroll
      for (int i = 0; i < 5; ++i) {
        const int kap = (rho & ~12) | ((rho & 4) << 1) | ((rho & 8) >> 1);
        const int part = (ch >= 10) + (ch >= 20);
        woff[i] = (unsigned)(kap * 3840 + part * (1280 - 160) + ch * 16);
        rho += 6; ch += 12;
        if (ch >= 30) { ch -= 30; rho += 1; }
      }
    }
    unsigned wl = sbase + lw * 1024, xl = sbase + K1_XB + lw * 1024;
    auto issueW = [&]() {
#pragma unroll
      for (int i = 0; i < 5; ++i) glds16(Wb, woff[i], wl + i * 3072);
      Wb += 32 * 3840;
      wl += K1_WH;
      if (wl >= sbase + K1_XB) wl -= K1_XB;
    };
    auto issueX = [&]() {
#pragma unroll
      for (int i = 0; i < 6; ++i) glds16(Xb, xoff[i], xl + i * 3072);
      Xb += 128;
      xl += K1_XST;
      if (xl >= sbase + K1_WPHI) xl -= 3 * K1_XST;
    };
    STAMP(1);
    issueX(); issueW(); issueW(); WAIT_V(5);
    STAMP(2);
    BAR();
    issueX(); issueW(); issueW(); WAIT_V(16); BAR();
    issueX(); issueW(); issueW(); WAIT_V(21); BAR();
    issueX(); issueW(); issueW(); WAIT_V(32); BAR();
#pragma unroll 1
    for (int i = 0; i < 6; ++i) {
      issueW(); WAIT_V(26); BAR();
      issueX(); issueW(); WAIT_V(32); BAR();
    }
    WAIT_V(21); BAR();
    WAIT_V(16); BAR();
    WAIT_V(5); BAR();
    WAIT_V(0); BAR();
    BAR();
  } else {
    const int th = w & 1;
    if (isB) {
      if (w == 0) k1_consumer<0, 0x60, true>(sbase, th, lane, ws);
      else if (w == 1) k1_consumer<0, 0x80, true>(sbase, th, lane, ws);
      else if (w == 2) k1_consumer<1, 0x0f, true>(sbase, th, lane, ws);
      else k1_consumer<1, 0x70, true>(sbase, th, lane, ws);
    } else {
      if (w < 2) k1_consumer<0, 0, false>(sbase, th, lane, ws);
      else k1_consumer<1, 0, false>(sbase, th, lane, ws);
    }
  }
  WAIT_L0();
  STAMP(4);
  s16x8 W0[5], W1[5];
  s16x4 W2[5];
  if (!isB) {
    const unsigned wpa = (pp == g) ? (sbase + K1_WPHI + g * 800 + qq * 40) : (sbase + K1_ZERO);
#pragma unroll
    for (int mo = 0; mo < 5; ++mo) {
      const unsigned a0 = wpa + mo * 8;
      W0[mo] = cat8(trr(a0), trr(a0 + 160));
      W1[mo] = cat8(trr(a0 + 320), trr(a0 + 480));
      W2[mo] = trr(a0 + 640);
    }
  }
  BAR();
  STAMP(5);

  if (!isB) {
    {
      const int tok = 16 * w + c;
      u32x2 in[2][5];
      {
        const unsigned qa = sbase + K1_QK + w * 5120 + lane * 16;
#pragma unroll
        for (int i = 0; i < 5; ++i) {
          const u32x4 v = *(LAS u32x4*)(qa + i * 1024);
          in[(2 * i) / 5][(2 * i) % 5] = (u32x2){v[0], v[1]};
          in[(2 * i + 1) / 5][(2 * i + 1) % 5] = (u32x2){v[2], v[3]};
        }
      }
      f32x4 P[2][5];
#pragma unroll
      for (int p = 1; p >= 0; --p) {
#pragma unroll
        for (int mo = 0; mo < 5; ++mo) P[p][mo] = mfma32(W0[mo], mk8(in[p][0], in[p][1]), (f32x4){0.f, 0.f, 0.f, 0.f});
#pragma unroll
        for (int mo = 0; mo < 5; ++mo) P[p][mo] = mfma32(W1[mo], mk8(in[p][2], in[p][3]), P[p][mo]);
#pragma unroll
        for (int mo = 0; mo < 5; ++mo) P[p][mo] = mfma16(W2[mo], __builtin_bit_cast(s16x4, in[p][4]), P[p][mo]);
      }
      u32x2* pqp = (u32x2*)(ws + WS_PQ + ((size_t)(hg * 4 + g) * 2048 + row0 + tok) * 40);
#pragma unroll
      for (int m = 0; m < 5; ++m) pqp[m] = (u32x2){pack2(P[0][m][0], P[0][m][1]), pack2(P[0][m][2], P[0][m][3])};
      float pv[20];
#pragma unroll
      for (int m = 0; m < 5; ++m)
#pragma unroll
        for (int r = 0; r < 4; ++r) pv[4 * m + r] = P[1][m][r];
      unsigned ph[20];
      dual_softmax(pv, ph);
      const unsigned rowa = sbase + (g * 128 + tok) * 160;
#pragma unroll
      for (int i = 0; i < 5; ++i) ldsw128(rowa + 16 * i, (u32x4){ph[4 * i], ph[4 * i + 1], ph[4 * i + 2], ph[4 * i + 3]});
    }
    WAIT_L0();
    BAR();
    {
      const int hl = w >> 1, jt = w & 1;
      const unsigned rbase = sbase + (hl * 128 + 4 * g + qq) * 160 + pp * 8;
      f32x4 kvacc[3];
#pragma unroll
      for (int it = 0; it < 3; ++it) kvacc[it] = (f32x4){0.f, 0.f, 0.f, 0.f};
#pragma unroll
      for (int ks = 0; ks < 4; ++ks) {
        const unsigned r0 = rbase + ks * 32 * 160;
        s16x8 Bv = cat8(trr(r0 + 80 + jt * 32), trr(r0 + 16 * 160 + 80 + jt * 32));
#pragma unroll
        for (int it = 0; it < 3; ++it) {
          s16x8 Ap = cat8(trr(r0 + it * 32), trr(r0 + 16 * 160 + it * 32));
          kvacc[it] = mfma32(Ap, Bv, kvacc[it]);
        }
      }
      const int d = jt * 16 + c;
      if (d < 21) {
        char* kp = ws + WS_KVP + ((size_t)(rt * 32 + hg * 4 + hl) * 21 + d) * 80;
#pragma unroll
        for (int it = 0; it < 3; ++it) {
          const int f = it * 16 + 4 * g;
          if (f < 40) *(u32x2*)(kp + f * 2) = (u32x2){pack2(kvacc[it][0], kvacc[it][1]), pack2(kvacc[it][2], kvacc[it][3])};
        }
      }
    }
  } else {
    const int T = w;
    const bool par = (c & 1) != 0;
    const bool cur0 = c - (4 * g + (c & 1)) >= 0, cur1 = c - (4 * g + 2 + (c & 1)) >= 0;
    const bool first = (rt == 0 && T == 0);
    const bool ok0 = cur0 || !first, ok1 = cur1 || !first;
    const unsigned hm = par ? 0xffff0000u : 0x0000ffffu;
    const unsigned mC0 = cur0 ? hm : 0u, mP0 = cur0 ? 0u : hm, mC1 = cur1 ? hm : 0u, mP1 = cur1 ? 0u : hm;
    const unsigned r0 = sbase + (16 * T) * 160;
    const unsigned ka = r0 + c * 160 + 64 + g * 16;
    const unsigned va = r0 + (4 * g + qq) * 160 + 104 + pp * 8;
    const f32x4 z4 = (f32x4){0.f, 0.f, 0.f, 0.f};
    f32x4 s0[4], s1[4];
    s16x8 V0[4], V1[4];
#pragma unroll
    for (int h = 0; h < 4; ++h) {
      s16x8 Kp = ldsr128(ka + h * 23040);
      s16x8 Kc = ldsr128(ka + h * 23040 + 2560);
      s16x8 Q = ldsr128(ka + h * 23040 + 2560 - 64);
      s0[h] = mfma32(Kp, Q, z4);
      s1[h] = mfma32(Kc, Q, z4);
    }
#pragma unroll
    for (int h = 0; h < 4; ++h) {
      V0[h] = cat8(trr(va + h * 23040), trr(va + h * 23040 + 2560));
      V1[h] = cat8(trr(va + h * 23040 + 32), trr(va + h * 23040 + 2560 + 32));
    }
    constexpr float SC = 0.22360679775f * LOG2E;
    float ea[4], eb[4], mx[4], sum[4];
#pragma unroll
    for (int h = 0; h < 4; ++h) {
      const float p0 = par ? s0[h][1] : s0[h][0], p1 = par ? s0[h][3] : s0[h][2];
      const float c0 = par ? s1[h][1] : s1[h][0], c1 = par ? s1[h][3] : s1[h][2];
      float a = cur0 ? c0 : p0, bq = cur1 ? c1 : p1;
      a = ok0 ? a : -3.0e38f;
      bq = ok1 ? bq : -3.0e38f;
      ea[h] = a; eb[h] = bq;
      mx[h] = fmaxf(a, bq);
    }
#pragma unroll
    for (int h = 0; h < 4; ++h) mx[h] = xmax4(mx[h]);
#pragma unroll
    for (int h = 0; h < 4; ++h) {
      const float bb = -mx[h] * SC;
      ea[h] = ok0 ? ex2(__builtin_fmaf(ea[h], SC, bb)) : 0.f;
      eb[h] = ok1 ? ex2(__builtin_fmaf(eb[h], SC, bb)) : 0.f;
      sum[h] = ea[h] + eb[h];
    }
#pragma unroll
    for (int h = 0; h < 4; ++h) sum[h] = xsum4(sum[h]);
    const unsigned ost = sbase + K1_OST + w * 2560;
#pragma unroll
    for (int h = 0; h < 4; ++h) {
      const float rs = rcp(sum[h]);
      const float pa = ea[h] * rs, pbv = eb[h] * rs;
      const unsigned da = pack2(pa, pa), db = pack2(pbv, pbv);
      u32x4 pb = {da & mP0, db & mP1, da & mC0, db & mC1};
      s16x8 Pb = __builtin_bit_cast(s16x8, pb);
      f32x4 o0 = mfma32(V0[h], Pb, z4);
      f32x4 o1 = mfma32(V1[h], Pb, z4);
      const int e0 = h * 20 + 4 * g, e1 = h * 20 + 16;
      ldsw64(ost + (e0 >> 3) * 256 + c * 16 + (e0 & 7) * 2, (u32x2){pack2(o0[0], o0[1]), pack2(o0[2], o0[3])});
      ldsw64(g == 0 ? ost + (e1 >> 3) * 256 + c * 16 + (e1 & 7) * 2 : sbase + K1_OST + 20480 + lane * 8,
             (u32x2){pack2(o1[0], o1[1]), pack2(o1[2], o1[3])});
    }
    WAIT_L0();
#pragma unroll
    for (int i = 0; i < 3; ++i) {
      const int e = i * 64 + lane;
      if (e < 160) {
        u32x4 v = *(LAS u32x4*)(ost + e * 16);
        *(u32x4*)(ws + WS_OB + (size_t)(rt * 8 + T) * 20480 + hg * 2560 + e * 16) = v;
      }
    }
  }
  STAMP(6);
}

__device__ __attribute__((noinline, used)) void code_mark_b() { asm volatile("s_nop 1"); }

extern "C" void kernel_launch(void* const* d_in, const int* in_sizes, int n_in, void* d_out, int out_size, void* d_ws,
                              size_t ws_size, hipStream_t stream) {
  const char* x = (const char*)d_in[0];
  const char* wA = (const char*)d_in[1];
  const char* wphi = (const char*)d_in[2];
  const char* woA = (const char*)d_in[3];
  const char* wB = (const char*)d_in[5];
  const char* woB = (const char*)d_in[6];
  const char* gw = (const char*)d_in[7];
  (void)hipFuncSetAttribute((const void*)k1_kernel, hipFuncAttributeMaxDynamicSharedMemorySize, K1_LDS);
  (void)hipFuncSetAttribute((const void*)k2_kernel, hipFuncAttributeMaxDynamicSharedMemorySize, K2_LDS);
  k1_kernel<<<256, 512, K1_LDS, stream>>>(x, wA, wB, wphi, gw, woA, woB, (char*)d_ws);
  k2_kernel<<<256, 640, K2_LDS, stream>>>((char*)d_ws, (char*)d_out);
}
```

```cpp
#include <hip/hip_runtime.h>
#include <hip/hip_bf16.h>
#include <stdint.h>

typedef __attribute__((ext_vector_type(4))) short s16x4;
typedef __attribute__((ext_vector_type(8))) short s16x8;
typedef __attribute__((ext_vector_type(8))) __bf16 bf16x8;
typedef __attribute__((ext_vector_type(2))) __bf16 bf16x2;
typedef __attribute__((ext_vector_type(2))) float f32x2;
typedef __attribute__((ext_vector_type(4))) float f32x4;
typedef __attribute__((ext_vector_type(2))) unsigned u32x2;
typedef __attribute__((ext_vector_type(4))) unsigned u32x4;
#define LAS __attribute__((address_space(3)))
#define GAS __attribute__((address_space(1)))
#define DI __device__ __forceinline__

__device__ __attribute__((noinline, used)) void code_mark_a() { asm volatile("s_nop 0"); }
__device__ __attribute__((noinline, used)) void code_mark_b();

constexpr size_t WS_CNT = 0;
constexpr size_t WS_GATE = 4096;
constexpr size_t WS_PQ = 16384;
constexpr size_t WS_OB = WS_PQ + 2621440;
constexpr size_t WS_OA = WS_OB + 2621440;
constexpr size_t WS_KVP = WS_OA + 2621440;
constexpr size_t WS_WO = WS_KVP + 860160;

constexpr float LOG2E = 1.4426950408889634f;

DI unsigned pack2(float a, float b) {
  f32x2 v = {a, b};
  bf16x2 r = __builtin_convertvector(v, bf16x2);
  return __builtin_bit_cast(unsigned, r);
}
DI float rb(float a) { return (float)(__bf16)a; }
DI float bflo(unsigned u) { return __uint_as_float(u << 16); }
DI float bfhi(unsigned u) { return __uint_as_float(u & 0xffff0000u); }
DI float ex2(float a) { return __builtin_amdgcn_exp2f(a); }
DI float rcp(float a) { return __builtin_amdgcn_rcpf(a); }
DI float dot2bf(unsigned a, unsigned b, float c) {
  return __builtin_amdgcn_fdot2_f32_bf16(__builtin_bit_cast(bf16x2, a), __builtin_bit_cast(bf16x2, b), c, false);
}

DI s16x4 trr(unsigned addr) { return __builtin_amdgcn_ds_read_tr16_b64_v4i16((LAS s16x4*)addr); }
DI s16x8 cat8(s16x4 a, s16x4 b) { return __builtin_shufflevector(a, b, 0, 1, 2, 3, 4, 5, 6, 7); }
DI s16x8 ldsr128(unsigned addr) { return *(LAS s16x8*)addr; }
DI s16x4 ldsr64(unsigned addr) { return *(LAS s16x4*)addr; }
DI void ldsw128(unsigned addr, u32x4 v) { *(LAS u32x4*)addr = v; }
DI void ldsw64(unsigned addr, u32x2 v) { *(LAS u32x2*)addr = v; }
DI f32x4 mfma32(s16x8 a, s16x8 b, f32x4 c) {
  return __builtin_amdgcn_mfma_f32_16x16x32_bf16(__builtin_bit_cast(bf16x8, a), __builtin_bit_cast(bf16x8, b), c, 0, 0, 0);
}
DI f32x4 mfma16(s16x4 a, s16x4 b, f32x4 c) {
  const s16x4 z = {0, 0, 0, 0};
  return mfma32(cat8(a, z), cat8(b, z), c);
}
DI s16x8 mk8(u32x2 a, u32x2 b) {
  u32x4 v = {a[0], a[1], b[0], b[1]};
  return __builtin_bit_cast(s16x8, v);
}
DI void glds16(const char* base, unsigned off, unsigned lds) {
  __builtin_amdgcn_global_load_lds(base + off, (LAS void*)lds, 16, 0, 0);
}
DI float xmax4(float v) {
  auto a = __builtin_amdgcn_permlane16_swap(__float_as_uint(v), __float_as_uint(v), false, false);
  v = fmaxf(__uint_as_float(a[0]), __uint_as_float(a[1]));
  auto b = __builtin_amdgcn_permlane32_swap(__float_as_uint(v), __float_as_uint(v), false, false);
  return fmaxf(__uint_as_float(b[0]), __uint_as_float(b[1]));
}
DI float xsum4(float v) {
  auto a = __builtin_amdgcn_permlane16_swap(__float_as_uint(v), __float_as_uint(v), false, false);
  v = __uint_as_float(a[0]) + __uint_as_float(a[1]);
  auto b = __builtin_amdgcn_permlane32_swap(__float_as_uint(v), __float_as_uint(v), false, false);
  return __uint_as_float(b[0]) + __uint_as_float(b[1]);
}
#define WAIT_V(n) asm volatile("s_waitcnt vmcnt(" #n ")" ::: "memory")
#define WAIT_L0() do { asm volatile("" ::: "memory"); __builtin_amdgcn_s_waitcnt(0xc07f); asm volatile("" ::: "memory"); } while (0)
#define BAR() do { asm volatile("" ::: "memory"); __builtin_amdgcn_s_barrier(); asm volatile("" ::: "memory"); } while (0)
#define SBAR() __builtin_amdgcn_sched_barrier(0)
#define SGB(mask, n) __builtin_amdgcn_sched_group_barrier(mask, n, 0)

#define ST_AUX 16
DI __amdgpu_buffer_rsrc_t ws_rsrc(char* ws) { return __builtin_amdgcn_make_buffer_rsrc(ws, 0, 16 << 20, 0x00020000); }
DI void st128(__amdgpu_buffer_rsrc_t r, unsigned off, u32x4 v) { __builtin_amdgcn_raw_buffer_store_b128(v, r, off, 0, ST_AUX); }
DI void st64(__amdgpu_buffer_rsrc_t r, unsigned off, u32x2 v) { __builtin_amdgcn_raw_buffer_store_b64(v, r, off, 0, ST_AUX); }

DI void dual_softmax(const float (&p)[20], unsigned (&ph)[20]) {
  float mx = p[0], mn = p[0];
#pragma unroll
  for (int i = 1; i < 20; ++i) { mx = fmaxf(mx, p[i]); mn = fminf(mn, p[i]); }
  const f32x2 bp = {-mx * LOG2E, -mx * LOG2E}, bn = {mn * LOG2E, mn * LOG2E};
  const f32x2 cp = {LOG2E, LOG2E}, cn = {-LOG2E, -LOG2E};
  f32x2 ep[10], en[10], sp = {0.f, 0.f}, sn = {0.f, 0.f};
#pragma unroll
  for (int i = 0; i < 10; ++i) {
    const f32x2 v = {p[2 * i], p[2 * i + 1]};
    const f32x2 a = __builtin_elementwise_fma(v, cp, bp), c = __builtin_elementwise_fma(v, cn, bn);
    ep[i] = (f32x2){ex2(a[0]), ex2(a[1])};
    en[i] = (f32x2){ex2(c[0]), ex2(c[1])};
    sp += ep[i];
    sn += en[i];
  }
  const float rp = rcp(sp[0] + sp[1]), rn = rcp(sn[0] + sn[1]);
#pragma unroll
  for (int i = 0; i < 10; ++i) {
    const f32x2 a = ep[i] * rp, c = en[i] * rn;
    ph[i] = pack2(a[0], a[1]);
    ph[10 + i] = pack2(c[0], c[1]);
  }
}

#define LD_DELAY 8
#define NB_EARLY 8
constexpr int K2_WST = 25600;
constexpr int K2_PHI = 5 * K2_WST;
constexpr int K2_RED = 2 * K2_WST;
constexpr int K2_KV = 6 * K2_WST;
constexpr int K2_FLG = K2_KV + 4096;
constexpr int K2_LDS = K2_FLG + 128;
constexpr int F_FULL = 0, F_FREE = 32, F_KV = 64, F_PA = 68, F_GO = 72, F_PUB = 76, F_RED = 80;

DI void lds_signal(unsigned addr, int lane) {
  if (lane == 0) asm volatile("ds_add_u32 %0, %1" :: "v"(addr), "v"(1u) : "memory");
}
DI void lds_wait(unsigned addr, unsigned want) {
#pragma nounroll
  for (unsigned spins = 0; spins < (1u << 24); ++spins) {
    unsigned v;
    asm volatile("ds_read_b32 %0, %1\n\ts_waitcnt lgkmcnt(0)" : "=v"(v) : "v"(addr) : "memory");
    if ((unsigned)__builtin_amdgcn_readfirstlane(v) >= want) break;
    __builtin_amdgcn_s_sleep(1);
  }
}

template <int S0, int NB>
DI void k2_loadA(unsigned wa0, s16x8 (&A)[NB][5], int n) {
  const int s = S0 + n / 5, ks = n % 5;
#pragma unroll
  for (int m = 0; m < 5; ++m) {
    A[n % NB][m] = ldsr128(wa0 + (s % 6) * K2_WST + ks * 5120 + m * 1024);
  }
}
DI void k2_gemm_b(unsigned wa0, unsigned fb, int lane, const u32x4 (&Bf)[20], f32x4 (&acc)[5]) {
  constexpr int NB = 3;
  s16x8 A[NB][5];
  lds_wait(fb + F_FULL + 0, 2);
  k2_loadA<0, NB>(wa0, A, 0);
  k2_loadA<0, NB>(wa0, A, 1);
#pragma unroll
  for (int n = 0; n < 20; ++n) {
    if (n + 2 < 20) {
      if ((n + 2) % 5 == 0) {
        lds_wait(fb + F_FULL + 4 * ((n + 2) / 5), 2);
        lds_signal(fb + F_FREE + 4 * ((n + 2) / 5 - 1), lane);
      }
      k2_loadA<0, NB>(wa0, A, n + 2);
    }
    SBAR();
#pragma unroll
    for (int m = 0; m < 5; ++m) acc[m] = mfma32(A[n % NB][m], __builtin_bit_cast(s16x8, Bf[n]), acc[m]);
    SBAR();
  }
}
template <int S0>
DI void k2_gemm_a(unsigned wa0, s16x8 (&A)[4][5], const u32x4 (&Bf)[10], f32x4 (&acc)[5]) {
#pragma unroll
  for (int n = 0; n < 10; ++n) {
#pragma unroll
    for (int m = 0; m < 5; ++m) acc[m] = mfma32(A[n % 4][m], __builtin_bit_cast(s16x8, Bf[n]), acc[m]);
    SBAR();
    if (n + 4 < 10) k2_loadA<S0, 4>(wa0, A, n + 4);
    SBAR();
  }
}

__global__ void __launch_bounds__(640, 3)
k2_kernel(char* __restrict__ ws, char* __restrict__ out) {
  extern __shared__ __attribute__((aligned(16))) char smem[];
  const unsigned sbase = (unsigned)(size_t)(LAS char*)smem;
  const int tid = threadIdx.x, lane = tid & 63;
  const int w = __builtin_amdgcn_readfirstlane(tid >> 6);
  const int b = blockIdx.x, xcd = b & 7, idx = b >> 3;
  const int rt2 = (xcd >> 1) * 8 + (idx & 7);
  const int ct = (xcd & 1) * 4 + (idx >> 3);
  const int g = lane >> 4, c = lane & 15, qq = (lane >> 2) & 3, pp = lane & 3;
  const int hp = xcd * 2 + (idx >> 4), tg = idx & 15;
  GAS unsigned* cnt = (GAS unsigned*)(ws + WS_CNT);
  const unsigned fb = sbase + K2_FLG;
  const unsigned wa0 = sbase + lane * 16;
  if (w == 8 && lane < 32) asm volatile("ds_write_b32 %0, %1\n\ts_waitcnt lgkmcnt(0)" :: "v"(fb + lane * 4), "v"(0u) : "memory");
  BAR();

  if (w >= 8) {
    const int lw = w - 8;
    const char* Wp = ws + WS_WO + (size_t)ct * 204800;
    const unsigned woff = (unsigned)(lw * 1024 + lane * 16);
    auto issueW = [&](int s) {
      const unsigned lb = sbase + (s % 6) * K2_WST + lw * 1024;
#pragma unroll
      for (int i = 0; i < 12; ++i) glds16(Wp + s * K2_WST, woff + i * 2048, lb + i * 2048);
      if (lw == 0) glds16(Wp + s * K2_WST, woff + 24576, lb + 24576);
    };
    __builtin_amdgcn_s_sleep(LD_DELAY);
    issueW(0); issueW(1); issueW(2);
    WAIT_V(24); lds_signal(fb + F_FULL + 0, lane);
    issueW(3);
    WAIT_V(24); lds_signal(fb + F_FULL + 4, lane);
    issueW(4);
    WAIT_V(24); lds_signal(fb + F_FULL + 8, lane);
    WAIT_V(12); lds_signal(fb + F_FULL + 12, lane);
    WAIT_V(0); lds_signal(fb + F_FULL + 16, lane);
    lds_wait(fb + F_FREE + 0, 4);
    issueW(6);
    lds_wait(fb + F_PA, 4);
    issueW(5);
    lds_wait(fb + F_FREE + 4, 4);
    issueW(7);
    WAIT_V(24); lds_signal(fb + F_FULL + 24, lane);
    WAIT_V(12); lds_signal(fb + F_FULL + 20, lane);
    WAIT_V(0); lds_signal(fb + F_FULL + 28, lane);
    return;
  }

  if (w >= 4) {
    __builtin_amdgcn_s_setprio(3);
    const int j = w - 4;
    const u32x2* pqp = (const u32x2*)(ws + WS_PQ + ((size_t)(hp * 2 + (lane >> 5)) * 2048 + tg * 128 + 32 * j + (lane & 31)) * 40);
    u32x2 pv[5];
#pragma unroll
    for (int i = 0; i < 5; ++i) pv[i] = pqp[i];
    float gta[2];
#pragma unroll
    for (int tt = 0; tt < 2; ++tt) gta[tt] = *(const float*)(ws + WS_GATE + (tg * 128 + 32 * j + 16 * tt + c) * 4);
    SBAR();
    const unsigned pb = sbase + K2_PHI + j * 5120;
    {
      float pk[20];
#pragma unroll
      for (int i = 0; i < 5; ++i) {
        pk[4 * i] = bflo(pv[i][0]); pk[4 * i + 1] = bfhi(pv[i][0]);
        pk[4 * i + 2] = bflo(pv[i][1]); pk[4 * i + 3] = bfhi(pv[i][1]);
      }
      unsigned ph[20];
      dual_softmax(pk, ph);
#pragma unroll
      for (int i = 0; i < 5; ++i) ldsw128(pb + lane * 80 + 16 * i, (u32x4){ph[4 * i], ph[4 * i + 1], ph[4 * i + 2], ph[4 * i + 3]});
    }
    WAIT_L0();
    s16x8 PB0[2][2];
    s16x4 PB1[2][2];
#pragma unroll
    for (int hh = 0; hh < 2; ++hh)
#pragma unroll
      for (int tt = 0; tt < 2; ++tt) {
        const unsigned pa = pb + (hh * 32 + tt * 16 + c) * 80;
        PB0[hh][tt] = ldsr128(pa + g * 16);
        s16x4 t1 = ldsr64(pa + 64 + (g & 1) * 8);
        const s16x4 z = {0, 0, 0, 0};
        PB1[hh][tt] = g < 2 ? t1 : z;
      }
    lds_wait(fb + F_KV, 4);
    const int r1 = c < 4 ? 16 + c : 20;
    const f32x4 z4 = (f32x4){0.f, 0.f, 0.f, 0.f};
    s16x8 KA0[2][2];
    s16x4 KA1[2][2];
#pragma unroll
    for (int hh = 0; hh < 2; ++hh) {
      const unsigned kb = sbase + K2_KV + hh * 21 * 96;
      KA0[hh][0] = ldsr128(kb + c * 96 + g * 16);
      KA0[hh][1] = ldsr128(kb + r1 * 96 + g * 16);
      KA1[hh][0] = ldsr64(kb + c * 96 + 64 + (g & 1) * 8);
      KA1[hh][1] = ldsr64(kb + r1 * 96 + 64 + (g & 1) * 8);
    }
    f32x4 n0[2][2], n1[2][2];
#pragma unroll
    for (int hh = 0; hh < 2; ++hh)
#pragma unroll
      for (int tt = 0; tt < 2; ++tt) {
        n1[hh][tt] = mfma32(KA0[hh][1], PB0[hh][tt], z4);
        n0[hh][tt] = mfma32(KA0[hh][0], PB0[hh][tt], z4);
      }
#pragma unroll
    for (int hh = 0; hh < 2; ++hh)
#pragma unroll
      for (int tt = 0; tt < 2; ++tt) {
        n1[hh][tt] = mfma16(KA1[hh][1], PB1[hh][tt], n1[hh][tt]);
        n0[hh][tt] = mfma16(KA1[hh][0], PB1[hh][tt], n0[hh][tt]);
      }
#pragma unroll
    for (int hh = 0; hh < 2; ++hh)
#pragma unroll
      for (int tt = 0; tt < 2; ++tt) {
        auto sw = __builtin_amdgcn_permlane16_swap(__float_as_uint(n1[hh][tt][0]), __float_as_uint(n1[hh][tt][0]), false, false);
        const float den = __uint_as_float(sw[1]);
        const float rd = gta[tt] * rcp(den + 1e-6f);
        const int e0 = hh * 20 + 4 * g, e1 = hh * 20 + 16;
        const unsigned oa = pb + tt * 1280 + c * 16;
        const f32x4 a = n0[hh][tt] * rd, bq = n1[hh][tt] * rd;
        ldsw64(oa + (e0 >> 3) * 256 + (e0 & 7) * 2, (u32x2){pack2(a[0], a[1]), pack2(a[2], a[3])});
        ldsw64(g == 0 ? oa + (e1 >> 3) * 256 + (e1 & 7) * 2 : pb + 4096 + lane * 8, (u32x2){pack2(bq[0], bq[1]), pack2(bq[2], bq[3])});
      }
    WAIT_L0();
    {
      const __amdgpu_buffer_rsrc_t rs = __builtin_amdgcn_make_buffer_rsrc(ws + WS_OA, 0, 2621440, 0x00020000);
      const unsigned gb = (unsigned)((tg * 8 + j * 2) * 20480 + hp * 1280);
      u32x4 v[3];
#pragma unroll
      for (int i = 0; i < 3; ++i) v[i] = *(LAS u32x4*)(pb + (i * 64 + lane) * 16);
#pragma unroll
      for (int i = 0; i < 3; ++i) {
        const int e = i * 64 + lane;
        if (e < 160) __builtin_amdgcn_raw_buffer_store_b128(v[i], rs, gb + (e >= 80 ? 20480 - 1280 : 0) + e * 16, 0, 16);
      }
      WAIT_L0();
      lds_signal(fb + F_PA, lane);
    }
    WAIT_V(0);
    if (lane == 0) {
      unsigned old;
      asm volatile("ds_add_rtn_u32 %0, %1, %2\n\ts_waitcnt lgkmcnt(0)" : "=v"(old) : "v"(fb + F_PUB), "v"(1u) : "memory");
      if (old == 3u) __hip_atomic_fetch_add(cnt + tg * 64, 1u, __ATOMIC_RELAXED, __HIP_MEMORY_SCOPE_AGENT);
    }
    __builtin_amdgcn_s_setprio(0);
    {
      const __amdgpu_buffer_rsrc_t rsa = __builtin_amdgcn_make_buffer_rsrc(ws + WS_OA, 0, 2621440, 0x00020000);
      const unsigned fo = (unsigned)((rt2 * 4 + j) * 20480 + lane * 16);
      f32x4 acc[5];
#pragma unroll
      for (int m = 0; m < 5; ++m) acc[m] = (f32x4){0.f, 0.f, 0.f, 0.f};
      s16x8 A[4][5];
      lds_wait(fb + F_FULL + 24, 2);
      lds_wait(fb + F_FULL + 28, 2);
#pragma unroll
      for (int n = 0; n < 4; ++n) k2_loadA<6, 4>(wa0, A, n);
      SBAR();
      if (j == 0) {
        GAS unsigned* cp = cnt + (rt2 >> 1) * 64;
        unsigned spins = 0;
        while ((unsigned)__builtin_amdgcn_readfirstlane(__hip_atomic_load(cp, __ATOMIC_RELAXED, __HIP_MEMORY_SCOPE_AGENT)) < 16u) {
          __builtin_amdgcn_s_sleep(2);
          if (++spins > (1u << 22)) break;
        }
        asm volatile("" ::: "memory");
        lds_signal(fb + F_GO, lane);
      } else {
        lds_wait(fb + F_GO, 1);
      }
      u32x4 Bf[10];
#pragma unroll
      for (int ks = 0; ks < 10; ++ks) Bf[ks] = __builtin_amdgcn_raw_buffer_load_b128(rsa, fo + (10 + ks) * 1024, 0, 16);
      SBAR();
      k2_gemm_a<6>(wa0, A, Bf, acc);
      lds_wait(fb + F_FREE + 8, 4);
#pragma unroll
      for (int m = 0; m < 5; ++m) *(LAS f32x4*)(sbase + K2_RED + j * 5120 + m * 1024 + lane * 16) = acc[m];
      WAIT_L0();
      lds_signal(fb + F_RED + 4 * j, lane);
    }
    return;
  }

  const unsigned fo = (unsigned)((rt2 * 4 + w) * 20480 + lane * 16);
  u32x4 Bf[20];
  {
    const __amdgpu_buffer_rsrc_t rk = __builtin_amdgcn_make_buffer_rsrc(ws + WS_KVP, 0, 860160, 0x00020000);
    const int t = tid < 210 ? tid : 209;
    u32x4 v[16];
#pragma unroll
    for (int r = 0; r < 16; ++r) v[r] = __builtin_amdgcn_raw_buffer_load_b128(rk, t * 16, hp * 3360 + r * 53760, 0);
    SBAR();
#pragma unroll
    for (int ks = 0; ks < NB_EARLY; ++ks) Bf[ks] = *(const u32x4*)(ws + WS_OB + fo + ks * 1024);
    SBAR();
    float a[8];
#pragma unroll
    for (int jj = 0; jj < 8; ++jj) a[jj] = 0.f;
#pragma unroll
    for (int r = 0; r < 16; ++r)
#pragma unroll
      for (int jj = 0; jj < 4; ++jj) { a[2 * jj] += bflo(v[r][jj]); a[2 * jj + 1] += bfhi(v[r][jj]); }
    const int row = (t * 52429) >> 18, f8 = t - row * 5;
    ldsw128(sbase + K2_KV + row * 96 + f8 * 16, (u32x4){pack2(a[0], a[1]), pack2(a[2], a[3]), pack2(a[4], a[5]), pack2(a[6], a[7])});
    WAIT_L0();
    lds_signal(fb + F_KV, lane);
    SBAR();
#pragma unroll
    for (int ks = NB_EARLY; ks < 20; ++ks) Bf[ks] = *(const u32x4*)(ws + WS_OB + fo + ks * 1024);
    SBAR();
  }
  const int tok = rt2 * 64 + 16 * w + c;
  const float gt = *(const float*)(ws + WS_GATE + tok * 4);
  f32x4 acc[5];
#pragma unroll
  for (int m = 0; m < 5; ++m) acc[m] = (f32x4){0.f, 0.f, 0.f, 0.f};
  k2_gemm_b(wa0, fb, lane, Bf, acc);
#pragma unroll
  for (int m = 0; m < 5; ++m) acc[m] *= (1.f - gt);
  {
    const __amdgpu_buffer_rsrc_t rsa = __builtin_amdgcn_make_buffer_rsrc(ws + WS_OA, 0, 2621440, 0x00020000);
    s16x8 A[4][5];
    lds_wait(fb + F_FULL + 16, 2);
    lds_wait(fb + F_FULL + 20, 2);
#pragma unroll
    for (int n = 0; n < 4; ++n) k2_loadA<4, 4>(wa0, A, n);
    SBAR();
    lds_wait(fb + F_GO, 1);
    u32x4 Ba[10];
#pragma unroll
    for (int ks = 0; ks < 10; ++ks) Ba[ks] = __builtin_amdgcn_raw_buffer_load_b128(rsa, fo + ks * 1024, 0, 16);
    SBAR();
    k2_gemm_a<4>(wa0, A, Ba, acc);
    lds_wait(fb + F_RED + 4 * w, 1);
#pragma unroll
    for (int m = 0; m < 5; ++m) acc[m] += *(LAS f32x4*)(sbase + K2_RED + w * 5120 + m * 1024 + lane * 16);
  }
  {
    char* op = out + ((size_t)tok * 640 + ct * 80 + 4 * g) * 2;
#pragma unroll
    for (int m = 0; m < 5; ++m) *(u32x2*)(op + m * 32) = (u32x2){pack2(acc[m][0], acc[m][1]), pack2(acc[m][2], acc[m][3])};
  }
}

constexpr int K1_WH = 15360;
constexpr int K1_XB = 6 * K1_WH;
constexpr int K1_XST = 18432;
constexpr int K1_WPHI = K1_XB + 3 * K1_XST;
constexpr int K1_ZERO = K1_WPHI + 4096;
constexpr int K1_RP = K1_ZERO + 1024;
constexpr int K1_LDS = K1_RP + 10240;
constexpr int K1_OST = 98304;
constexpr int K1_QK = 81920;

constexpr int ftoff(int ft) { return (ft / 5) * 160 + (ft % 5) * 8; }

template <int FH, int HM>
DI void k1_consumer_main(unsigned sbase, int th, int lane, f32x4 (&acc)[8][4], f32x4 (&acch)[8], char* ws) {
  constexpr int NT = FH ? 7 : 8;
  const int g = lane >> 4, c = lane & 15, qq = (lane >> 2) & 3, pp = lane & 3;
  const unsigned wa0 = sbase + (16 * (g >> 1) + 4 * (g & 1) + qq) * 480 + pp * 40;
  unsigned xh0[2], xb0[2];
#pragma unroll
  for (int a = 0; a < 2; ++a) {
    xh0[a] = sbase + K1_XB + c * 128 + (((4 * a + g) ^ (c & 7)) * 16);
    xb0[a] = xh0[a] + (16 + 64 * th) * 128;
  }
#pragma unroll
  for (int m = 0; m < 8; ++m) {
#pragma unroll
    for (int t = 0; t < 4; ++t) acc[m][t] = (f32x4){0.f, 0.f, 0.f, 0.f};
    acch[m] = (f32x4){0.f, 0.f, 0.f, 0.f};
  }
  s16x8 A[2][4], B[2][4], BH[2];
  auto loadA = [&](int grp, unsigned woff) {
#pragma unroll
    for (int m = 0; m < 4; ++m)
      if (4 * grp + m < NT) {
        const unsigned ad = wa0 + woff + ftoff(8 * FH + 4 * grp + m);
        A[grp][m] = cat8(trr(ad), trr(ad + 3840));
      }
  };
  auto loadB = [&](int buf, unsigned xoff) {
#pragma unroll
    for (int t = 0; t < 4; ++t) B[buf][t] = ldsr128(xb0[buf] + xoff + t * 2048);
    if (HM) BH[buf] = ldsr128(xh0[buf] + xoff);
  };
  auto mm = [&](int grp, int buf) {
#pragma unroll
    for (int m = 0; m < 4; ++m)
      if (4 * grp + m < NT) {
#pragma unroll
        for (int t = 0; t < 4; ++t) acc[4 * grp + m][t] = mfma32(A[grp][m], B[buf][t], acc[4 * grp + m][t]);
        if ((HM >> (4 * grp + m)) & 1) acch[4 * grp + m] = mfma32(A[grp][m], BH[buf], acch[4 * grp + m]);
      }
  };
  auto kstep = [&](int a, unsigned woff, unsigned nwoff, unsigned nxoff, bool last) {
    loadA(1, woff);
    mm(0, a);
    SGB(0x8, 1); SGB(0x100, 2); SGB(0x8, 1); SGB(0x100, 2); SGB(0x8, 1); SGB(0x100, 2); SGB(0x8, 1); SGB(0x100, 2);
    SBAR();
    WAIT_L0();
    BAR();
    if (!last) {
      loadB(a ^ 1, nxoff);
      loadA(0, nwoff);
    }
    mm(1, a);
    if (!last) {
      SGB(0x8, 1); SGB(0x100, 2); SGB(0x8, 1); SGB(0x100, 2); SGB(0x8, 1); SGB(0x100, 2); SGB(0x8, 1); SGB(0x100, 2);
      SGB(0x8, 1); SGB(0x100, 2); SGB(0x8, 1); SGB(0x100, 2); SGB(0x8, 1); SGB(0x100, 1);
      SBAR();
    }
  };
  BAR();
  loadB(0, 0);
  loadA(0, 0);
  SBAR();
  unsigned woff = 0, xoff = 0;
#pragma unroll 1
  for (int s = 0; s < 9; ++s) {
    unsigned nw = woff + 2 * K1_WH, nx = xoff + K1_XST;
    if (nw >= 6 * K1_WH) { nw = 0; nx = 0; }
    kstep(0, woff, woff + K1_WH, xoff, false);
    kstep(1, woff + K1_WH, nw, nx, false);
    woff = nw; xoff = nx;
  }
  kstep(0, 0, K1_WH, 0, false);
  kstep(1, K1_WH, 0, 0, true);
}

template <int FH, int HM, bool ISB>
DI void k1_dump(unsigned sbase, int th, int lane, const f32x4 (&acc)[8][4], const f32x4 (&acch)[8]) {
  constexpr int NT = FH ? 7 : 8;
  const int g = lane >> 4, c = lane & 15;
#pragma unroll
  for (int t = 0; t < 4; ++t) {
    u32x2 v[8];
#pragma unroll
    for (int m = 0; m < NT; ++m) v[m] = (u32x2){pack2(acc[m][t][0], acc[m][t][1]), pack2(acc[m][t][2], acc[m][t][3])};
    const int tok = 64 * th + 16 * t + c;
    if (ISB) {
      const unsigned rowa = sbase + (g * 144 + 16 + tok) * 160;
      if (FH == 0) {
        ldsw128(rowa + 0, (u32x4){v[0][0], v[0][1], v[1][0], v[1][1]});
        ldsw128(rowa + 16, (u32x4){v[2][0], v[2][1], v[3][0], v[3][1]});
        ldsw128(rowa + 32, (u32x4){v[4][0], v[4][1], 0u, 0u});
        ldsw128(rowa + 48, (u32x4){0u, 0u, 0u, 0u});
        ldsw128(rowa + 64, (u32x4){v[5][0], v[5][1], v[6][0], v[6][1]});
        ldsw64(rowa + 80, v[7]);
      } else {
        ldsw64(rowa + 88, v[0]);
        ldsw128(rowa + 96, (u32x4){v[1][0], v[1][1], v[2][0], v[2][1]});
        ldsw128(rowa + 112, (u32x4){v[3][0], v[3][1], v[4][0], v[4][1]});
        ldsw128(rowa + 128, (u32x4){v[5][0], v[5][1], v[6][0], v[6][1]});
      }
    } else {
      const unsigned qa = sbase + K1_QK + (4 * th + t) * 5120 + lane * 16;
      if (FH == 0) {
#pragma unroll
        for (int i = 0; i < 4; ++i) ldsw128(qa + i * 1024, (u32x4){v[2 * i][0], v[2 * i][1], v[2 * i + 1][0], v[2 * i + 1][1]});
      } else {
        ldsw128(qa + 4096, (u32x4){v[0][0], v[0][1], v[1][0], v[1][1]});
        const unsigned rowa = sbase + (g * 128 + tok) * 160;
        ldsw128(rowa + 80, (u32x4){v[2][0], v[2][1], v[3][0], v[3][1]});
        ldsw128(rowa + 96, (u32x4){v[4][0], v[4][1], v[5][0], v[5][1]});
        ldsw128(rowa + 112, (u32x4){v[6][0], v[6][1], 0x00003f80u, 0u});
      }
    }
  }
  if (ISB) {
#pragma unroll
    for (int m = 0; m < NT; ++m)
      if ((HM >> m) & 1)
        ldsw64(sbase + (g * 144 + c) * 160 + 64 + (8 * FH + m - 5) * 8, (u32x2){pack2(acch[m][0], acch[m][1]), pack2(acch[m][2], acch[m][3])});
  }
}

template <int FH, int HM, bool ISB>
DI void k1_consumer(unsigned sbase, int th, int lane, char* ws) {
  f32x4 acc[8][4], acch[8];
  k1_consumer_main<FH, HM>(sbase, th, lane, acc, acch, ws);
  k1_dump<FH, HM, ISB>(sbase, th, lane, acc, acch);
}

__global__ void __launch_bounds__(512, 2)
k1_kernel(const char* __restrict__ x, const char* __restrict__ wA, const char* __restrict__ wB,
          const char* __restrict__ wphi, const char* __restrict__ gatew, const char* __restrict__ woA,
          const char* __restrict__ woB, char* __restrict__ ws) {
  extern __shared__ __attribute__((aligned(16))) char smem[];
  const unsigned sbase = (unsigned)(size_t)(LAS char*)smem;
  const int tid = threadIdx.x, lane = tid & 63;
  const int w = __builtin_amdgcn_readfirstlane(tid >> 6);
  const int b = blockIdx.x, xcd = b & 7, idx = b >> 3;
  const int rt = (xcd & 1) * 8 + (idx & 7);
  const int cg = (xcd >> 1) * 4 + (idx >> 3);
  const bool isB = cg >= 8;
  const int hg = cg & 7;
  const int row0 = rt * 128;
  const int g = lane >> 4, c = lane & 15, qq = (lane >> 2) & 3, pp = lane & 3;
  if (w == 7) {
    {
      const unsigned dl = sbase + K1_LDS;
      const unsigned lo16 = (unsigned)lane * 16u;
#pragma unroll
      for (int i = 0; i < 10; ++i) {
        glds16(x + (size_t)b * 10240, lo16 + i * 1024, dl);
        const unsigned o = i < 9 ? lo16 + i * 1024 : (lo16 < 384u ? lo16 : 368u) + 9216u;
        glds16(wA + (size_t)b * 9600, o, dl);
        glds16(wB + (size_t)b * 9600, o, dl);
      }
    }
    unsigned pfc;
    {
      const uintptr_t pa = (uintptr_t)&code_mark_a, pb2 = (uintptr_t)&code_mark_b;
      const uintptr_t lo = pa < pb2 ? pa : pb2, hi = pa < pb2 ? pb2 : pa;
      uintptr_t ad = (lo & ~(uintptr_t)127) + (uintptr_t)(idx * 16 + (lane & 15)) * 128;
      ad = (ad >= lo && ad < hi) ? ad : lo;
      pfc = *(const GAS unsigned*)(ad & ~(uintptr_t)3);
    }
    SBAR();
    BAR(); BAR(); BAR(); BAR();
#pragma unroll
    for (int i = 0; i < 4; ++i) {
      int e = i * 64 + lane;
      e = e > 199 ? 199 : e;
      glds16(wphi + hg * 3200, (unsigned)e * 16u, sbase + K1_WPHI + i * 1024);
    }
    asm volatile("ds_write_b128 %0, %1" :: "v"(sbase + K1_ZERO + lane * 16), "v"((u32x4){0u, 0u, 0u, 0u}) : "memory");
    if (b == 0 && lane < 16) *(unsigned*)(ws + WS_CNT + lane * 256) = 0u;
    BAR();
#pragma unroll
    for (int r = 0; r < 2; ++r) {
      const int u = r == 0 ? b : (b < 64 ? 256 + b : b);
      const int uct = u / 40, uks = u - uct * 40;
      const char* wsrc = (uks < 20 ? woB + (size_t)uks * 40960 : woA + (size_t)(uks - 20) * 40960) + uct * 160;
#pragma unroll
      for (int i = 0; i < 5; ++i) {
        const int e = i * 64 + lane;
        const int rho = (e * 6554) >> 16, ch = e - rho * 10;
        const int kap = (rho & ~12) | ((rho & 4) << 1) | ((rho & 8) >> 1);
        glds16(wsrc, (unsigned)(kap * 1280 + ch * 16), sbase + K1_RP + r * 5120 + i * 1024);
      }
      BAR();
    }
    const int gtok = row0 + cg * 8 + (lane >> 3);
    u32x4 xv[10], gv[10];
#pragma unroll
    for (int i = 0; i < 10; ++i) gv[i] = *(const u32x4*)(gatew + (lane & 7) * 160 + i * 16);
    SBAR();
    BAR();
#pragma unroll
    for (int i = 0; i < 10; ++i) xv[i] = *(const u32x4*)(x + (size_t)gtok * 1280 + (lane & 7) * 160 + i * 16);
    SBAR();
    BAR(); BAR(); BAR(); BAR();
    SBAR();
    WAIT_V(0);
    asm volatile("" :: "v"(pfc));
    {
      float a0 = 0.f, a1 = 0.f;
#pragma unroll
      for (int i = 0; i < 10; ++i)
#pragma unroll
        for (int j = 0; j < 4; j += 2) {
          a0 = dot2bf(xv[i][j], gv[i][j], a0);
          a1 = dot2bf(xv[i][j + 1], gv[i][j + 1], a1);
        }
      float a = a0 + a1;
      BAR();
      a += __int_as_float(__builtin_amdgcn_update_dpp(0, __float_as_int(a), 0xB1, 0xf, 0xf, true));
      a += __int_as_float(__builtin_amdgcn_update_dpp(0, __float_as_int(a), 0x4E, 0xf, 0xf, true));
      a += __int_as_float(__builtin_amdgcn_update_dpp(0, __float_as_int(a), 0x141, 0xf, 0xf, true));
      if ((lane & 7) == 0) *(float*)(ws + WS_GATE + gtok * 4) = rcp(1.f + ex2(-a * LOG2E));
      BAR();
    }
#pragma unroll
    for (int r = 0; r < 2; ++r) {
      const int u = r == 0 ? b : 256 + b;
      const unsigned ra = sbase + K1_RP + r * 5120 + (16 * (g >> 1) + 4 * (g & 1) + qq) * 160 + pp * 8;
      s16x8 F[5];
#pragma unroll
      for (int m = 0; m < 5; ++m) F[m] = cat8(trr(ra + m * 32), trr(ra + m * 32 + 1280));
      BAR();
      if (r == 0 || b < 64) {
#pragma unroll
        for (int m = 0; m < 5; ++m)
          st128(ws_rsrc(ws), (unsigned)(WS_WO + u * 5120 + m * 1024 + lane * 16), __builtin_bit_cast(u32x4, F[m]));
      }
      BAR();
    }
    BAR();
    WAIT_L0();
    BAR(); BAR();
  } else if (w >= 4) {
    const int lw = w - 4;
    const char* Wb = (isB ? wB : wA) + hg * 160;
    const char* Xb = x;
    unsigned xoff[6], woff[5];
    {
      const int l8 = lane >> 3, slot = lane & 7;
      const unsigned sw = (unsigned)((slot ^ l8) * 16);
#pragma unroll
      for (int i = 0; i < 6; ++i) {
        int tok = row0 - 16 + (3 * i + lw) * 8 + l8;
        tok = tok < 0 ? 0 : tok;
        xoff[i] = (unsigned)tok * 1280u + sw;
      }
    }
    {
      const int u0 = lw * 64 + lane;
      int rho = (u0 * 2185) >> 16;
      int ch = u0 - rho * 30;
#pragma unroll
      for (int i = 0; i < 5; ++i) {
        const int kap = (rho & ~12) | ((rho & 4) << 1) | ((rho & 8) >> 1);
        const int part = (ch >= 10) + (ch >= 20);
        woff[i] = (unsigned)(kap * 3840 + part * (1280 - 160) + ch * 16);
        rho += 6; ch += 12;
        if (ch >= 30) { ch -= 30; rho += 1; }
      }
    }
    unsigned wl = sbase + lw * 1024, xl = sbase + K1_XB + lw * 1024;
    auto issueW = [&]() {
#pragma unroll
      for (int i = 0; i < 5; ++i) glds16(Wb, woff[i], wl + i * 3072);
      Wb += 32 * 3840;
      wl += K1_WH;
      if (wl >= sbase + K1_XB) wl -= K1_XB;
    };
    auto issueX = [&]() {
#pragma unroll
      for (int i = 0; i < 6; ++i) glds16(Xb, xoff[i], xl + i * 3072);
      Xb += 128;
      xl += K1_XST;
      if (xl >= sbase + K1_WPHI) xl -= 3 * K1_XST;
    };
    issueX(); issueW(); issueW(); WAIT_V(5);
    BAR();
    issueX(); issueW(); issueW(); WAIT_V(16); BAR();
    issueX(); issueW(); issueW(); WAIT_V(21); BAR();
    issueX(); issueW(); issueW(); WAIT_V(32); BAR();
#pragma unroll 1
    for (int i = 0; i < 6; ++i) {
      issueW(); WAIT_V(26); BAR();
      issueX(); issueW(); WAIT_V(32); BAR();
    }
    WAIT_V(21); BAR();
    WAIT_V(16); BAR();
    WAIT_V(5); BAR();
    WAIT_V(0); BAR();
    BAR();
  } else {
    const int th = w & 1;
    if (isB) {
      if (w == 0) k1_consumer<0, 0x60, true>(sbase, th, lane, ws);
      else if (w == 1) k1_consumer<0, 0x80, true>(sbase, th, lane, ws);
      else if (w == 2) k1_consumer<1, 0x0f, true>(sbase, th, lane, ws);
      else k1_consumer<1, 0x70, true>(sbase, th, lane, ws);
    } else {
      if (w < 2) k1_consumer<0, 0, false>(sbase, th, lane, ws);
      else k1_consumer<1, 0, false>(sbase, th, lane, ws);
    }
  }
  WAIT_L0();
  s16x8 W0[5], W1[5];
  s16x4 W2[5];
  if (!isB) {
    const unsigned wpa = (pp == g) ? (sbase + K1_WPHI + g * 800 + qq * 40) : (sbase + K1_ZERO);
#pragma unroll
    for (int mo = 0; mo < 5; ++mo) {
      const unsigned a0 = wpa + mo * 8;
      W0[mo] = cat8(trr(a0), trr(a0 + 160));
      W1[mo] = cat8(trr(a0 + 320), trr(a0 + 480));
      W2[mo] = trr(a0 + 640);
    }
  }
  BAR();

  if (!isB) {
    {
      const int tok = 16 * w + c;
      u32x2 in[2][5];
      {
        const unsigned qa = sbase + K1_QK + w * 5120 + lane * 16;
#pragma unroll
        for (int i = 0; i < 5; ++i) {
          const u32x4 v = *(LAS u32x4*)(qa + i * 1024);
          in[(2 * i) / 5][(2 * i) % 5] = (u32x2){v[0], v[1]};
          in[(2 * i + 1) / 5][(2 * i + 1) % 5] = (u32x2){v[2], v[3]};
        }
      }
      f32x4 P[2][5];
#pragma unroll
      for (int p = 1; p >= 0; --p) {
#pragma unroll
        for (int mo = 0; mo < 5; ++mo) P[p][mo] = mfma32(W0[mo], mk8(in[p][0], in[p][1]), (f32x4){0.f, 0.f, 0.f, 0.f});
#pragma unroll
        for (int mo = 0; mo < 5; ++mo) P[p][mo] = mfma32(W1[mo], mk8(in[p][2], in[p][3]), P[p][mo]);
#pragma unroll
        for (int mo = 0; mo < 5; ++mo) P[p][mo] = mfma16(W2[mo], __builtin_bit_cast(s16x4, in[p][4]), P[p][mo]);
      }
      u32x2* pqp = (u32x2*)(ws + WS_PQ + ((size_t)(hg * 4 + g) * 2048 + row0 + tok) * 40);
#pragma unroll
      for (int m = 0; m < 5; ++m) pqp[m] = (u32x2){pack2(P[0][m][0], P[0][m][1]), pack2(P[0][m][2], P[0][m][3])};
      float pv[20];
#pragma unroll
      for (int m = 0; m < 5; ++m)
#pragma unroll
        for (int r = 0; r < 4; ++r) pv[4 * m + r] = P[1][m][r];
      unsigned ph[20];
      dual_softmax(pv, ph);
      const unsigned rowa = sbase + (g * 128 + tok) * 160;
#pragma unroll
      for (int i = 0; i < 5; ++i) ldsw128(rowa + 16 * i, (u32x4){ph[4 * i], ph[4 * i + 1], ph[4 * i + 2], ph[4 * i + 3]});
    }
    WAIT_L0();
    BAR();
    {
      const int hl = w >> 1, jt = w & 1;
      const unsigned rbase = sbase + (hl * 128 + 4 * g + qq) * 160 + pp * 8;
      f32x4 kvacc[3];
#pragma unroll
      for (int it = 0; it < 3; ++it) kvacc[it] = (f32x4){0.f, 0.f, 0.f, 0.f};
#pragma unroll
      for (int ks = 0; ks < 4; ++ks) {
        const unsigned r0 = rbase + ks * 32 * 160;
        s16x8 Bv = cat8(trr(r0 + 80 + jt * 32), trr(r0 + 16 * 160 + 80 + jt * 32));
#pragma unroll
        for (int it = 0; it < 3; ++it) {
          s16x8 Ap = cat8(trr(r0 + it * 32), trr(r0 + 16 * 160 + it * 32));
          kvacc[it] = mfma32(Ap, Bv, kvacc[it]);
        }
      }
      const int d = jt * 16 + c;
      if (d < 21) {
        char* kp = ws + WS_KVP + ((size_t)(rt * 32 + hg * 4 + hl) * 21 + d) * 80;
#pragma unroll
        for (int it = 0; it < 3; ++it) {
          const int f = it * 16 + 4 * g;
          if (f < 40) *(u32x2*)(kp + f * 2) = (u32x2){pack2(kvacc[it][0], kvacc[it][1]), pack2(kvacc[it][2], kvacc[it][3])};
        }
      }
    }
  } else {
    const int T = w;
    const bool par = (c & 1) != 0;
    const bool cur0 = c - (4 * g + (c & 1)) >= 0, cur1 = c - (4 * g + 2 + (c & 1)) >= 0;
    const bool first = (rt == 0 && T == 0);
    const bool ok0 = cur0 || !first, ok1 = cur1 || !first;
    const unsigned hm = par ? 0xffff0000u : 0x0000ffffu;
    const unsigned mC0 = cur0 ? hm : 0u, mP0 = cur0 ? 0u : hm, mC1 = cur1 ? hm : 0u, mP1 = cur1 ? 0u : hm;
    const unsigned r0 = sbase + (16 * T) * 160;
    const unsigned ka = r0 + c * 160 + 64 + g * 16;
    const unsigned va = r0 + (4 * g + qq) * 160 + 104 + pp * 8;
    const f32x4 z4 = (f32x4){0.f, 0.f, 0.f, 0.f};
    f32x4 s0[4], s1[4];
    s16x8 V0[4], V1[4];
#pragma unroll
    for (int h = 0; h < 4; ++h) {
      s16x8 Kp = ldsr128(ka + h * 23040);
      s16x8 Kc = ldsr128(ka + h * 23040 + 2560);
      s16x8 Q = ldsr128(ka + h * 23040 + 2560 - 64);
      s0[h] = mfma32(Kp, Q, z4);
      s1[h] = mfma32(Kc, Q, z4);
    }
#pragma unroll
    for (int h = 0; h < 4; ++h) {
      V0[h] = cat8(trr(va + h * 23040), trr(va + h * 23040 + 2560));
      V1[h] = cat8(trr(va + h * 23040 + 32), trr(va + h * 23040 + 2560 + 32));
    }
    constexpr float SC = 0.22360679775f * LOG2E;
    float ea[4], eb[4], mx[4], sum[4];
#pragma unroll
    for (int h = 0; h < 4; ++h) {
      const float p0 = par ? s0[h][1] : s0[h][0], p1 = par ? s0[h][3] : s0[h][2];
      const float c0 = par ? s1[h][1] : s1[h][0], c1 = par ? s1[h][3] : s1[h][2];
      float a = cur0 ? c0 : p0, bq = cur1 ? c1 : p1;
      a = ok0 ? a : -3.0e38f;
      bq = ok1 ? bq : -3.0e38f;
      ea[h] = a; eb[h] = bq;
      mx[h] = fmaxf(a, bq);
    }
#pragma unroll
    for (int h = 0; h < 4; ++h) mx[h] = xmax4(mx[h]);
#pragma unroll
    for (int h = 0; h < 4; ++h) {
      const float bb = -mx[h] * SC;
      ea[h] = ok0 ? ex2(__builtin_fmaf(ea[h], SC, bb)) : 0.f;
      eb[h] = ok1 ? ex2(__builtin_fmaf(eb[h], SC, bb)) : 0.f;
      sum[h] = ea[h] + eb[h];
    }
#pragma unroll
    for (int h = 0; h < 4; ++h) sum[h] = xsum4(sum[h]);
    const unsigned ost = sbase + K1_OST + w * 2560;
#pragma unroll
    for (int h = 0; h < 4; ++h) {
      const float rs = rcp(sum[h]);
      const float pa = ea[h] * rs, pbv = eb[h] * rs;
      const unsigned da = pack2(pa, pa), db = pack2(pbv, pbv);
      u32x4 pb = {da & mP0, db & mP1, da & mC0, db & mC1};
      s16x8 Pb = __builtin_bit_cast(s16x8, pb);
      f32x4 o0 = mfma32(V0[h], Pb, z4);
      f32x4 o1 = mfma32(V1[h], Pb, z4);
      const int e0 = h * 20 + 4 * g, e1 = h * 20 + 16;
      ldsw64(ost + (e0 >> 3) * 256 + c * 16 + (e0 & 7) * 2, (u32x2){pack2(o0[0], o0[1]), pack2(o0[2], o0[3])});
      ldsw64(g == 0 ? ost + (e1 >> 3) * 256 + c * 16 + (e1 & 7) * 2 : sbase + K1_OST + 20480 + lane * 8,
             (u32x2){pack2(o1[0], o1[1]), pack2(o1[2], o1[3])});
    }
    WAIT_L0();
#pragma unroll
    for (int i = 0; i < 3; ++i) {
      const int e = i * 64 + lane;
      if (e < 160) {
        u32x4 v = *(LAS u32x4*)(ost + e * 16);
        *(u32x4*)(ws + WS_OB + (size_t)(rt * 8 + T) * 20480 + hg * 2560 + e * 16) = v;
      }
    }
  }
}

__device__ __attribute__((noinline, used)) void code_mark_b() { asm volatile("s_nop 1"); }

extern "C" void kernel_launch(void* const* d_in, const int* in_sizes, int n_in, void* d_out, int out_size, void* d_ws,
                              size_t ws_size, hipStream_t stream) {
  const char* x = (const char*)d_in[0];
  const char* wA = (const char*)d_in[1];
  const char* wphi = (const char*)d_in[2];
  const char* woA = (const char*)d_in[3];
  const char* wB = (const char*)d_in[5];
  const char* woB = (const char*)d_in[6];
  const char* gw = (const char*)d_in[7];
  (void)hipFuncSetAttribute((const void*)k1_kernel, hipFuncAttributeMaxDynamicSharedMemorySize, K1_LDS + 1024);
  (void)hipFuncSetAttribute((const void*)k2_kernel, hipFuncAttributeMaxDynamicSharedMemorySize, K2_LDS);
  k1_kernel<<<256, 512, K1_LDS + 1024, stream>>>(x, wA, wB, wphi, gw, woA, woB, (char*)d_ws);
  k2_kernel<<<256, 640, K2_LDS, stream>>>((char*)d_ws, (char*)d_out);
}
```
